```python
import torch

def _softmax(x, axis=-1):
    return torch.softmax(x, dim=axis)
def _gelu(x):
    return torch.nn.functional.gelu(x, approximate="tanh")
def _silu(x):
    return torch.nn.functional.silu(x)
def _sigmoid(x):
    return torch.sigmoid(x)
def _layernorm(x, axis=-1, eps=1e-5):
    m = x.mean(dim=axis, keepdim=True)
    v = x.var(dim=axis, keepdim=True, unbiased=False)
    return (x - m) / torch.sqrt(v + eps)
def _scatter_add(tgt, idx, src, axis=0):
    return tgt.clone().index_add_(axis, idx, src)
def _segment_sum(src, seg, n):
    out = torch.zeros((n,) + src.shape[1:], dtype=src.dtype)
    return out.index_add_(0, seg, src)
def _pad(x, pw):
    flat = tuple(v for pair in reversed(pw) for v in pair)
    return torch.nn.functional.pad(x, flat)

kernel_name = 'recurrent_scan__linear_attn_block__mixer_s_mlp__prompt'

def setup_inputs():

    _w = (lambda a: (lambda t: t.to(torch.bfloat16) if t.is_floating_point() else t)(torch.as_tensor(a)))
    def _A_setup():
        B, S, D = 1, 2048, 512
        return {
            "x": _w((lambda *s: torch.randn(*s, dtype=torch.bfloat16) * 0.05)(B, S, D)),
            "wqkv": _w((lambda *s: torch.randn(*s, dtype=torch.bfloat16) * 0.05)(D, 3 * D)),
            "wo": _w((lambda *s: torch.randn(*s, dtype=torch.bfloat16) * 0.05)(D, D)),
        }
    _A_in = _A_setup()

    def _B_setup():
        B, S, D, F = 1, 2048, 512, 1792
        return {
            "x":  _w((lambda *s: torch.randn(*s, dtype=torch.bfloat16) * 0.05)(B, S, D)),
            "w1": _w((lambda *s: torch.randn(*s, dtype=torch.bfloat16) * 3.2)(D, D) * (D ** -0.5)),
            "w2": _w((lambda *s: torch.randn(*s, dtype=torch.bfloat16) * 3.2)(D, D) * (D ** -0.5)),
            "w3": _w((lambda *s: torch.randn(*s, dtype=torch.bfloat16) * 3.2)(D, F) * (D ** -0.5)),
            "w4": _w((lambda *s: torch.randn(*s, dtype=torch.bfloat16) * 1.6)(F, D) * (F ** -0.5)),
        }
    _B_in = _B_setup()

    return {
        "A_x": _A_in["x"],
        "A_wqkv": _A_in["wqkv"],
        "A_wo": _A_in["wo"],
        "B_x": _B_in["x"],
        "B_w1": _B_in["w1"],
        "B_w2": _B_in["w2"],
        "B_w3": _B_in["w3"],
        "B_w4": _B_in["w4"],
    }

def reference(A_x, A_wqkv, A_wo, B_x, B_w1, B_w2, B_w3, B_w4):
    def _A_ref(x, wqkv, wo):
        B, S, D = x.shape
        H = 32; Dh = D // H
        qkv = (lambda x, *d: x.permute(*d))((x @ wqkv).reshape(B, S, 3, H, Dh), 2, 0, 3, 1, 4)
        q, k, v = qkv[0], qkv[1], qkv[2]
        phi = lambda t: torch.where(t > 0, t + 1, torch.exp(t)) + 1e-6
        pq, pk = phi(q), phi(k)
        kv = torch.einsum("bhsd,bhse->bhde", pk, v)
        z = torch.einsum("bhsd->bhd", pk)
        num = torch.einsum("bhqd,bhde->bhqe", pq, kv)
        den = torch.einsum("bhqd,bhd->bhq", pq, z)[..., None] + 1e-6
        o = num / den
        return (lambda x, *d: x.permute(*d))(o, 0, 2, 1, 3).reshape(B, S, D) @ wo

    def _B_ref(x, w1, w2, w3, w4):
        B, S, D = x.shape
        h = _gelu(_layernorm(x, axis=-1) @ w1)
        c = D // 4
        g0 = _pad(h[:, :, 0*c:1*c], ((0, 0), (1, 0), (0, 0)))[:, :S, :]
        g1 = _pad(h[:, :, 1*c:2*c], ((0, 0), (0, 1), (0, 0)))[:, 1:, :]
        g2 = _pad(h[:, :, 2*c:3*c], ((0, 0), (2, 0), (0, 0)))[:, :S, :]
        g3 = _pad(h[:, :, 3*c:   ], ((0, 0), (0, 2), (0, 0)))[:, 2:, :]
        hs = torch.concatenate([g0, g1, g2, g3], axis=-1) @ w2
        x1 = x + hs
        h2 = _gelu(_layernorm(x1, axis=-1) @ w3) @ w4
        return x1 + h2

    x = A_x
    xn = _layernorm(x, axis=-1)
    s = _A_ref(xn, A_wqkv, A_wo)
    g = _silu(_B_ref(xn, B_w1, B_w2, B_w3, B_w4))
    return x + s * g
```

```cpp
#include <hip/hip_runtime.h>
#include <hip/hip_bf16.h>
#include <stdint.h>

typedef unsigned short u16;
static __device__ __forceinline__ float b2f(u16 b) { return __uint_as_float(((unsigned)b) << 16); }
static __device__ __forceinline__ u16 f2b(float f) { __hip_bfloat16 h = __float2bfloat16(f); return *(u16*)&h; }
static __device__ __forceinline__ float rb(float f) { return b2f(f2b(f)); }

constexpr int S = 2048, D = 512, F = 1792, H = 32, DH = 16;

__global__ void k_ln(const u16* __restrict__ x, u16* __restrict__ y) {
  int row = blockIdx.x; int l = threadIdx.x;
  float v[8]; float s = 0;
  for (int i = 0; i < 8; i++) { v[i] = b2f(x[row * D + l * 8 + i]); s += v[i]; }
  for (int o = 32; o; o >>= 1) s += __shfl_xor(s, o);
  float m = s / D; float q = 0;
  for (int i = 0; i < 8; i++) { float d = v[i] - m; q += d * d; }
  for (int o = 32; o; o >>= 1) q += __shfl_xor(q, o);
  float r = rsqrtf(q / D + 1e-5f);
  for (int i = 0; i < 8; i++) y[row * D + l * 8 + i] = f2b((v[i] - m) * r);
}
static __device__ __forceinline__ float gelu(float x) {
  float u = 0.7978845608028654f * (x + 0.044715f * x * x * x);
  return 0.5f * x * (1.f + tanhf(u));
}
template <int MODE, int SHIFT>
__global__ void k_gemm(const u16* __restrict__ A, const u16* __restrict__ W, u16* __restrict__ C, int N, int K) {
  __shared__ float As[32][33], Ws[32][33];
  int tx = threadIdx.x, ty = threadIdx.y;
  int row = blockIdx.y * 32 + ty, col = blockIdx.x * 32 + tx;
  float acc = 0;
  for (int k0 = 0; k0 < K; k0 += 32) {
    int ar = row;
    if (SHIFT) { int g = (k0 + tx) / 128; ar = row + (g == 0 ? -1 : g == 1 ? 1 : g == 2 ? -2 : 2); }
    As[ty][tx] = (ar >= 0 && ar < S) ? b2f(A[(size_t)ar * K + k0 + tx]) : 0.f;
    Ws[ty][tx] = b2f(W[(size_t)(k0 + ty) * N + col]);
    __syncthreads();
    for (int k = 0; k < 32; k++) acc += As[ty][k] * Ws[k][tx];
    __syncthreads();
  }
  if (MODE == 1) acc = gelu(rb(acc));
  C[(size_t)row * N + col] = f2b(acc);
}
static __device__ __forceinline__ float phi(float t) { return (t > 0 ? t + 1.f : __expf(t)) + 1e-6f; }
__global__ void k_kv(const u16* __restrict__ qkv, float* __restrict__ kv) {
  int h = blockIdx.x; int t = threadIdx.x;
  float acc = 0;
  if (t < 256) { int d = t / 16, e = t % 16;
    for (int s = 0; s < S; s++) acc += rb(phi(b2f(qkv[s * 1536 + 512 + h * 16 + d]))) * b2f(qkv[s * 1536 + 1024 + h * 16 + e]);
  } else if (t < 272) { int d = t - 256;
    for (int s = 0; s < S; s++) acc += rb(phi(b2f(qkv[s * 1536 + 512 + h * 16 + d])));
  }
  if (t < 272) kv[h * 272 + t] = acc;
}
__global__ void k_o(const u16* __restrict__ qkv, const float* __restrict__ kv, u16* __restrict__ o) {
  int s = blockIdx.x; int c = threadIdx.x; int h = c / 16, e = c % 16;
  float num = 0, den = 0;
  for (int d = 0; d < 16; d++) { float p = rb(phi(b2f(qkv[s * 1536 + h * 16 + d]))); num += p * kv[h * 272 + d * 16 + e]; den += p * kv[h * 272 + 256 + d]; }
  o[s * D + c] = f2b(num / (den + 1e-6f));
}
__global__ void k_add(const u16* a, const u16* b, u16* c) { int i = blockIdx.x * 256 + threadIdx.x; c[i] = f2b(b2f(a[i]) + b2f(b[i])); }
__global__ void k_fin(const u16* x, const u16* s, const u16* x1, const u16* h2, u16* out) {
  int i = blockIdx.x * 256 + threadIdx.x;
  float y = rb(b2f(x1[i]) + b2f(h2[i]));
  float g = rb(y / (1.f + __expf(-y)));
  out[i] = f2b(b2f(x[i]) + rb(b2f(s[i]) * g));
}

extern "C" void kernel_launch(void* const* d_in, const int* in_sizes, int n_in,
                              void* d_out, int out_size, void* d_ws, size_t ws_size,
                              hipStream_t stream) {
  const u16* x = (const u16*)d_in[0]; const u16* wqkv = (const u16*)d_in[1]; const u16* wo = (const u16*)d_in[2];
  const u16* w1 = (const u16*)d_in[4]; const u16* w2 = (const u16*)d_in[5]; const u16* w3 = (const u16*)d_in[6]; const u16* w4 = (const u16*)d_in[7];
  u16* ws = (u16*)d_ws; size_t SD = (size_t)S * D;
  u16* xn = ws; u16* xn2 = ws + SD; u16* qkv = ws + 2 * SD; u16* h = ws + 5 * SD; u16* hs = ws + 6 * SD; u16* x1 = ws + 7 * SD;
  u16* x1n = ws + 8 * SD; u16* h3 = ws + 9 * SD; u16* h2 = ws + 13 * SD; u16* o = ws + 14 * SD; u16* s = ws + 15 * SD; float* kv = (float*)(ws + 16 * SD);
  dim3 b(32, 32);
  k_ln<<<S, 64, 0, stream>>>(x, xn);
  k_ln<<<S, 64, 0, stream>>>(xn, xn2);
  k_gemm<0, 0><<<dim3(1536 / 32, S / 32), b, 0, stream>>>(xn, wqkv, qkv, 1536, 512);
  k_kv<<<H, 320, 0, stream>>>(qkv, kv);
  k_o<<<S, 512, 0, stream>>>(qkv, kv, o);
  k_gemm<0, 0><<<dim3(512 / 32, S / 32), b, 0, stream>>>(o, wo, s, 512, 512);
  k_gemm<1, 0><<<dim3(512 / 32, S / 32), b, 0, stream>>>(xn2, w1, h, 512, 512);
  k_gemm<0, 1><<<dim3(512 / 32, S / 32), b, 0, stream>>>(h, w2, hs, 512, 512);
  k_add<<<SD / 256, 256, 0, stream>>>(xn, hs, x1);
  k_ln<<<S, 64, 0, stream>>>(x1, x1n);
  k_gemm<1, 0><<<dim3(F / 32, S / 32), b, 0, stream>>>(x1n, w3, h3, F, 512);
  k_gemm<0, 0><<<dim3(512 / 32, S / 32), b, 0, stream>>>(h3, w4, h2, 512, F);
  k_fin<<<SD / 256, 256, 0, stream>>>(x, s, x1, h2, (u16*)d_out);
}
```

```cpp
#include <hip/hip_runtime.h>
#include <stdint.h>
#pragma clang diagnostic ignored "-Wint-to-pointer-cast"

#define OUTAUX 0
#define NPRE 16
#define NPRE4 16
#define KVFL 16
#define EARLY4 1
#define WIN4 10
#define T1AT 0
#define T2AT 13
#define TMODE 1
#define PS1 6
#define PC0 9
#define PS_X1 13
#define MAP 0
#define FPRIO 0
#define PI0 1
#define PIM 1
#define NPRB 1
#define PT0 3
#define PT1 0
#define SC3S 0
#define FLIP 1
#define XROT 3
#define KVT0 1
#define SC3A 3
#define HORD 1
#define PS_A 14
#define PS_B 16
#define PS_C 4
#define PS_D 8
#define OST 1
#define OUTM 1
#define C16 0
#define XW 16
#define T4SL 10
#define OUTT 0
#define CP4 0
#define WA 5
#define WB 6
#define SELFH 0
#define XAT 1
#define T6SL 0
#define SELF2 0
#define PCKF 26
#define PCKP 18
#define KVEV 5
#define LNORD 1
#define PS_O 16
#define PCO 30
#define KVST 1
#define KVEARLY 1
#define PS_KP 8
#define SILU 5
#define RDYAT 3
#define CPRIO 0
#define OPRIO 0
#define WSL 0

namespace {
typedef unsigned u32;
typedef unsigned long long u64;
typedef u32 v4u __attribute__((ext_vector_type(4)));
typedef u32 v2u __attribute__((ext_vector_type(2)));
typedef float f32x2 __attribute__((ext_vector_type(2)));
typedef float f32x4 __attribute__((ext_vector_type(4)));
typedef float f32x16 __attribute__((ext_vector_type(16)));
typedef short s16x4 __attribute__((ext_vector_type(4)));
typedef __bf16 bf16x2 __attribute__((ext_vector_type(2)));
typedef __bf16 bf16x8 __attribute__((ext_vector_type(8)));
#define LAS __attribute__((address_space(3)))
#define GAS __attribute__((address_space(1)))
#define DEV __device__ __forceinline__
#define RLX_AGENT __ATOMIC_RELAXED, __HIP_MEMORY_SCOPE_AGENT
typedef __amdgpu_buffer_rsrc_t rsrc_t;

template <int N> struct IC { static constexpr int value = N; };
template <int B, int E, class F> DEV void sfor(F&& f) {
  if constexpr (B < E) { f(IC<B>{}); sfor<B + 1, E>(f); }
}

constexpr u32 WS_EPOCH = 0;
constexpr u32 WS_KVF = 0x8000;
constexpr u32 WS_FG = 0x10000;
constexpr int F_KP = 0, F_KF = 4096;
constexpr u32 WS_KVP = 0x40000;
constexpr u32 WS_CL = 0x400000, CL = 0x100000;
constexpr u32 sp(u32 o) { return o + (o & ~8191u); }
constexpr u32 C_H = 0, C_X1 = sp(0x10000), C_O = sp(0x20000), C_ST = sp(0x30000), C_FL = sp(0x32000), C_PR = sp(0x34000), C_H2 = sp(0x40000);
constexpr u32 WS_END = 0x2800000;
constexpr u32 OOB = 0x7ff00000u;
constexpr int SC1 = 16;

constexpr int PROW = 1040;
constexpr int L_R1 = 70720, L_R1L = L_R1 + 288, L_R12 = L_R1 + 576;
constexpr int L_NM3 = L_R1L, L_R3 = L_NM3 + 256;
constexpr int L_SYNC = 71600;
constexpr int Y_WARM = 0, Y_STAT = 16, Y_HALO = 32, Y_RDYA = 48, Y_RDYB = 64;
constexpr int Y_CONS = 80, Y_LN = 96, Y_XCH = 112, Y_GO = 128, Y_PI = 144;
constexpr int L_EV = L_SYNC + 464;
constexpr int L_LAND = 72704;
constexpr int L_HX = 73728;
constexpr int L_KF = 65536;
constexpr int L_RING = 81920;
constexpr int NPIECE = 52;
constexpr int slot_of(int p) {
  return p < 5 ? L_RING + 16384 * p : p == 18 || p == 19 ? slot_of(p - 4) : p == 20 ? slot_of(13) : p <= 28 ? slot_of(p - 5)
       : p <= 32 ? 16384 * (p - 29) : slot_of(p - 9);
}
constexpr int line_of(int i) { constexpr int t[14] = {3, 10, 0, 4, 7, 11, 1, 5, 8, 12, 2, 6, 9, 13}; return t[i]; }
constexpr u32 SPIN_LDS = 1u << 20, SPIN_GLB = 1u << 16;

DEV u32 f2u(float f) { return __builtin_bit_cast(u32, f); }
DEV float u2f(u32 u) { return __builtin_bit_cast(float, u); }
DEV u32 pk2(float a, float b) { f32x2 v = {a, b}; return __builtin_bit_cast(u32, __builtin_convertvector(v, bf16x2)); }
DEV float blo(u32 d) { return u2f(d << 16); }
DEV float bhi(u32 d) { return u2f(d & 0xffff0000u); }
DEV f32x16 mfma(v4u a, v4u b, f32x16 c) {
  return __builtin_amdgcn_mfma_f32_32x32x16_bf16(__builtin_bit_cast(bf16x8, a), __builtin_bit_cast(bf16x8, b), c, 0, 0, 0);
}
DEV v4u lds128(u32 a) { return *(LAS const v4u*)a; }
DEV void lds128w(u32 a, v4u v) { *(LAS v4u*)a = v; }
DEV v4u tr2(u32 a0, u32 a1) {
  v2u x = __builtin_bit_cast(v2u, __builtin_amdgcn_ds_read_tr16_b64_v4i16((LAS s16x4*)a0));
  v2u y = __builtin_bit_cast(v2u, __builtin_amdgcn_ds_read_tr16_b64_v4i16((LAS s16x4*)a1));
  return v4u{x.x, x.y, y.x, y.y};
}
DEV u32 pin(u32 v) { asm volatile("" : "+v"(v)); return v; }
struct LB { u32 b[3]; };
DEV LB mklb(u32 v) { LB r; r.b[0] = pin(v); r.b[1] = pin(v + 65536); r.b[2] = pin(v + 131072); return r; }
template <int OFF> DEV u32 at(const LB& b) { return b.b[OFF >> 16] + (OFF & 65535); }
DEV rsrc_t mkrs(const void* p, u32 bytes) { return __builtin_amdgcn_make_buffer_rsrc((void*)p, 0, bytes, 0x00020000); }
template <int AUX> DEV void st128(v4u d, rsrc_t r, u32 vo, u32 so) {
  __builtin_amdgcn_raw_buffer_store_b128(d, r, vo, so, AUX);
  asm volatile("s_nop 1" :: "v"(d) : "memory");
}
DEV void fence_sched() { __builtin_amdgcn_sched_barrier(0); }
DEV void drain() { asm volatile("s_waitcnt vmcnt(0)" ::: "memory"); }

template <int OFF, int WANT, int SLEEP> DEV void spin(u32 va) {
  u32 t, c;
  asm volatile(
      "s_mov_b32 %1, %5\n"
      "0:\n\t"
      "ds_read_b32 %0, %2 offset:%3\n\t"
      "s_waitcnt lgkmcnt(0)\n\t"
      "v_cmp_gt_u32 vcc, %4, %0\n\t"
      "s_cbranch_vccz 1f\n\t"
      "s_sleep %6\n\t"
      "s_sub_u32 %1, %1, 1\n\t"
      "s_cbranch_scc0 0b\n"
      "1:"
      : "=&v"(t), "=&s"(c) : "v"(va), "i"(OFF), "i"(WANT), "i"(SPIN_LDS), "i"(SLEEP) : "memory", "vcc", "scc");
}
template <int OFF, int WANT> DEV void spin_pre(u32 va, u32 pre) {
  u32 t, c;
  asm volatile(
      "v_cmp_gt_u32 vcc, %4, %6\n\t"
      "s_cbranch_vccnz 2f\n"
      "1:\n\t"
      ".subsection 1\n"
      "2:\n\t"
      "s_mov_b32 %1, %5\n"
      "0:\n\t"
      "ds_read_b32 %0, %2 offset:%3\n\t"
      "s_waitcnt lgkmcnt(0)\n\t"
      "v_cmp_gt_u32 vcc, %4, %0\n\t"
      "s_cbranch_vccz 1b\n\t"
      "s_sub_u32 %1, %1, 1\n\t"
      "s_cbranch_scc0 0b\n\t"
      "s_branch 1b\n\t"
      ".subsection 0"
      : "=&v"(t), "=&s"(c) : "v"(va), "i"(OFF), "i"(WANT), "i"(SPIN_LDS), "v"(pre) : "memory", "vcc", "scc");
}
template <int P> DEV u32 peek_l(u32 vl) { return *(volatile LAS u32*)(vl + 4 * P); }
DEV u64 chk1(u32 pre) { u64 m; asm volatile("v_cmp_ne_u32_e64 %0, 0, %1" : "=s"(m) : "v"(pre)); return m; }
template <int P> DEV void chk2(u32 vl, u64 m
    ) {
  u32 t, c;
  asm volatile(
      "s_cmp_lg_u64 %[m], 0\n\t"
      "s_cbranch_scc1 2f\n"
      "1:\n\t"
      ".subsection 1\n"
      "2:\n\t"
      "s_mov_b32 %[c], %[spin]\n"
      "0:\n\t"
      "ds_read_b32 %[t], %[vl] offset:%[off]\n\t"
      "s_waitcnt lgkmcnt(0)\n\t"
      "v_cmp_ne_u32 vcc, 0, %[t]\n\t"
      "s_cbranch_vccz 1b\n\t"
      "s_sub_u32 %[c], %[c], 1\n\t"
      "s_cbranch_scc0 0b\n\t"
      "s_branch 1b\n\t"
      ".subsection 0"
      : [t] "=&v"(t), [c] "=&s"(c)
      : [vl] "v"(vl), [off] "i"(4 * P), [want] "i"(P + 1), [spin] "i"(SPIN_LDS), [m] "s"(m) : "memory", "vcc", "scc");
}
template <int P, int SLEEP> DEV void spin_l(u32 vl) {
  u32 t, c;
  asm volatile(
      "s_mov_b32 %1, %4\n"
      "0:\n\t"
      "ds_read_b32 %0, %2 offset:%3\n\t"
      "s_waitcnt lgkmcnt(0)\n\t"
      "v_cmp_ne_u32 vcc, 0, %0\n\t"
      "s_cbranch_vccz 1f\n\t"
      "s_sleep %5\n\t"
      "s_sub_u32 %1, %1, 1\n\t"
      "s_cbranch_scc0 0b\n"
      "1:"
      : "=&v"(t), "=&s"(c) : "v"(vl), "i"(4 * P), "i"(SPIN_LDS), "i"(SLEEP) : "memory", "vcc", "scc");
}
DEV void zero_behind(rsrc_t r, u32 at, u32 oob) {
  asm volatile("s_mov_b32 m0, %1\n\ts_nop 0\n\tbuffer_load_dword %2, %0, 0 offen lds" :: "s"(r), "s"(at), "v"(oob) : "memory");
}
template <int OFF> DEV void count(u32 a, u32& n) {
  asm volatile("v_add_u32 %0, 1, %0" : "+v"(n) :: "memory");
  *(volatile LAS u32*)(a + OFF) = n;
  asm volatile("" ::: "memory");
}
template <int OFF> DEV u32 peek(u32 va) { return *(volatile LAS u32*)(va + OFF); }
template <int OFF, int VAL> DEV void post(u32 a) {
  u32 t;
  asm volatile("v_mov_b32 %0, %3\n\tds_write_b32 %1, %0 offset:%2" : "=&v"(t) : "v"(a), "i"(OFF), "i"(VAL) : "memory");
}

#define STAMP(i)
#define STAMP_DECL
#define STAMP_OUT(ws, w)

template <int CTRL> DEV float dpp_add(float v) {
  return v + u2f((u32)__builtin_amdgcn_update_dpp(0, (int)f2u(v), CTRL, 0xf, 0xf, true));
}
DEV float swap16_add(float v) { auto r = __builtin_amdgcn_permlane16_swap(f2u(v), f2u(v), false, false); return u2f(r[0]) + u2f(r[1]); }
DEV float swap32_add(float v) { auto r = __builtin_amdgcn_permlane32_swap(f2u(v), f2u(v), false, false); return u2f(r[0]) + u2f(r[1]); }
template <int N> DEV void row_sum(float (&v)[N]) {
  sfor<0, N>([&](auto I) { v[I.value] = dpp_add<0x128>(v[I.value]); });
  sfor<0, N>([&](auto I) { v[I.value] = dpp_add<0x124>(v[I.value]); });
  sfor<0, N>([&](auto I) { v[I.value] = dpp_add<0x122>(v[I.value]); });
  sfor<0, N>([&](auto I) { v[I.value] = dpp_add<0x121>(v[I.value]); });
}
DEV float dot2(u32 a, u32 b, float c) { return __builtin_amdgcn_fdot2_f32_bf16(__builtin_bit_cast(bf16x2, a), __builtin_bit_cast(bf16x2, b), c, false); }
DEV float sum32(const v4u (&d)[4]) {
  float s[2] = {0.f, 0.f};
  sfor<0, 16>([&](auto I) { constexpr int i = I.value; s[i & 1] = dot2(d[i >> 2][i & 3], 0x3f803f80u, s[i & 1]); });
  return s[0] + s[1];
}
DEV float sq32(const v4u (&d)[4]) {
  float s[2] = {0.f, 0.f};
  sfor<0, 16>([&](auto I) { constexpr int i = I.value; s[i & 1] = dot2(d[i >> 2][i & 3], d[i >> 2][i & 3], s[i & 1]); });
  return s[0] + s[1];
}
DEV v4u center8(v4u d, float nm) {
  float t0, t1, t2, t3, t4, t5, t6, t7;
  u32 o0, o1, o2, o3;
  asm("v_dot2_f32_bf16 %4, %12, %16, %18\n\t"
      "v_dot2_f32_bf16 %5, %12, %17, %18\n\t"
      "v_dot2_f32_bf16 %6, %13, %16, %18\n\t"
      "v_dot2_f32_bf16 %7, %13, %17, %18\n\t"
      "v_dot2_f32_bf16 %8, %14, %16, %18\n\t"
      "v_dot2_f32_bf16 %9, %14, %17, %18\n\t"
      "v_dot2_f32_bf16 %10, %15, %16, %18\n\t"
      "v_dot2_f32_bf16 %11, %15, %17, %18\n\t"
      "v_cvt_pk_bf16_f32 %0, %4, %5\n\t"
      "v_cvt_pk_bf16_f32 %1, %6, %7\n\t"
      "v_cvt_pk_bf16_f32 %2, %8, %9\n\t"
      "v_cvt_pk_bf16_f32 %3, %10, %11"
      : "=v"(o0), "=v"(o1), "=v"(o2), "=v"(o3), "=&v"(t0), "=&v"(t1), "=&v"(t2), "=&v"(t3), "=&v"(t4), "=&v"(t5), "=&v"(t6), "=&v"(t7)
      : "v"(d.x), "v"(d.y), "v"(d.z), "v"(d.w), "s"(0x00003f80u), "s"(0x3f800000u), "v"(nm));
  return v4u{o0, o1, o2, o3};
}
struct C8 { float t[8]; };
DEV void center_a(v4u d, float nm, float (&t)[8]) {
  asm volatile(
      "v_dot2_f32_bf16 %0, %8, %12, %14\n\t"
      "v_dot2_f32_bf16 %1, %8, %13, %14\n\t"
      "v_dot2_f32_bf16 %2, %9, %12, %14\n\t"
      "v_dot2_f32_bf16 %3, %9, %13, %14\n\t"
      "v_dot2_f32_bf16 %4, %10, %12, %14\n\t"
      "v_dot2_f32_bf16 %5, %10, %13, %14\n\t"
      "v_dot2_f32_bf16 %6, %11, %12, %14\n\t"
      "v_dot2_f32_bf16 %7, %11, %13, %14"
      : "=&v"(t[0]), "=&v"(t[1]), "=&v"(t[2]), "=&v"(t[3]), "=&v"(t[4]), "=&v"(t[5]), "=&v"(t[6]), "=&v"(t[7])
      : "v"(d.x), "v"(d.y), "v"(d.z), "v"(d.w), "s"(0x00003f80u), "s"(0x3f800000u), "v"(nm));
}
DEV v4u center_b(const float (&t)[8]) {
  u32 o0, o1, o2, o3;
  asm volatile(
      "v_cvt_pk_bf16_f32 %0, %4, %5\n\t"
      "v_cvt_pk_bf16_f32 %1, %6, %7\n\t"
      "v_cvt_pk_bf16_f32 %2, %8, %9\n\t"
      "v_cvt_pk_bf16_f32 %3, %10, %11"
      : "=&v"(o0), "=&v"(o1), "=&v"(o2), "=&v"(o3)
      : "v"(t[0]), "v"(t[1]), "v"(t[2]), "v"(t[3]), "v"(t[4]), "v"(t[5]), "v"(t[6]), "v"(t[7]));
  return v4u{o0, o1, o2, o3};
}
constexpr float LOG2E = 1.4426950408889634f;
DEV void put_scales(u32 lb, u32 prow, int k, float var) {
  const float r = __builtin_amdgcn_rsqf(var + 1e-5f);
  const float r2 = __builtin_amdgcn_rsqf(__builtin_fmaf(var * r, r, 1e-5f));
  const float f = k == 0 ? 1.0f : k == 1 ? LOG2E : r2;
  *(LAS float*)(lb + L_R1 + (k < 2 ? k : 2) * 288 + (prow + 2) * 4) = r * f;
}
typedef f32x4 GK;
DEV GK gelu_k(float r) {
  constexpr float C1 = -2.0f * 0.7978845608028654f * LOG2E, C2 = C1 * 0.044715f;
  return GK{C1 * r, C2 * r * r * r, __builtin_amdgcn_rcpf(r), 0.f};
}
template <int B> DEV v4u gelu8(const f32x16& a, GK k) {
  f32x2 x[4], t[4];
  const f32x2 k1 = {k.x, k.x}, k2 = {k.y, k.y}, ir = {k.z, k.z};
  sfor<0, 4>([&](auto I) { constexpr int i = I.value; x[i] = f32x2{a[B + 2 * i], a[B + 2 * i + 1]}; t[i] = x[i] * x[i]; });
  sfor<0, 4>([&](auto I) { constexpr int i = I.value; t[i] = __builtin_elementwise_fma(t[i], k2, k1); });
  sfor<0, 4>([&](auto I) { constexpr int i = I.value; t[i] *= x[i]; });
  sfor<0, 4>([&](auto I) { constexpr int i = I.value; t[i] = f32x2{__builtin_amdgcn_exp2f(t[i].x), __builtin_amdgcn_exp2f(t[i].y)}; });
  sfor<0, 4>([&](auto I) { constexpr int i = I.value; t[i] = __builtin_elementwise_fma(t[i], ir, ir); });
  sfor<0, 4>([&](auto I) { constexpr int i = I.value; t[i] = f32x2{__builtin_amdgcn_rcpf(t[i].x), __builtin_amdgcn_rcpf(t[i].y)}; });
  sfor<0, 4>([&](auto I) { constexpr int i = I.value; t[i] *= x[i]; });
  return v4u{pk2(t[0].x, t[0].y), pk2(t[1].x, t[1].y), pk2(t[2].x, t[2].y), pk2(t[3].x, t[3].y)};
}
DEV float a_add(float a, float b) { float r; asm volatile("v_add_f32 %0, %1, %2" : "=v"(r) : "v"(a), "v"(b)); return r; }
DEV float a_mul(float a, float b) { float r; asm volatile("v_mul_f32 %0, %1, %2" : "=v"(r) : "v"(a), "v"(b)); return r; }
DEV float a_exp(float a) { float r; asm volatile("v_exp_f32 %0, %1" : "=v"(r) : "v"(a)); return r; }
DEV float a_rcp(float a) { float r; asm volatile("v_rcp_f32 %0, %1" : "=v"(r) : "v"(a)); return r; }
DEV float phi(float a, float r, float rl) {
  const float e = __builtin_amdgcn_fmed3f(__builtin_amdgcn_exp2f(a * rl), 0.f, 1.f);
  return __builtin_amdgcn_fmed3f(__builtin_fmaf(a, r, 1e-6f), 1e-6f, __builtin_inff()) + e;
}
template <int B> DEV v4u pack8(const f32x16& a) { return v4u{pk2(a[B], a[B + 1]), pk2(a[B + 2], a[B + 3]), pk2(a[B + 4], a[B + 5]), pk2(a[B + 6], a[B + 7])}; }
template <int B> DEV v4u raw4(const f32x16& a) { return v4u{f2u(a[B]), f2u(a[B + 1]), f2u(a[B + 2]), f2u(a[B + 3])}; }

struct Sy { u32 va, vn, vl;
  mutable u32 nc, nl, nx;
};
template <int K> DEV void prof(const Sy&) {}
template <int P0, int LAST> struct Plain {
  static constexpr int want(int n) { return (n & 7) ? 0 : P0 + (n >> 3) + 1; }
  static constexpr bool done(int n) { return (n & 7) == 7 && P0 + (n >> 3) < LAST; }
  static constexpr int first = P0;
};
template <int P0, int NU, int LAST, int N0> struct Tail {
  static constexpr int want(int n) { return n <= NU - 16 ? Plain<P0, LAST>::want(n) : n == NU - 12 ? P0 + NU / 8 : 0; }
  static constexpr bool done(int n) { return n < NU - 16 ? Plain<P0, LAST>::done(n) : false; }
  static constexpr bool own(int n) { return n < NU - 16 ? (n & 1) : n >= NU - 8; }
  static constexpr int ks(int n) { return n < NU - 16 ? n >> 1 : (NU - 16) / 2 + (n & 7); }
  static constexpr int R(int pos) { return pos < N0 ? N0 : pos < NU ? pos : NU; }
  static constexpr int first = P0;
};
constexpr int imin(int a, int b) { return a < b ? a : b; }
constexpr int imax(int a, int b) { return a > b ? a : b; }
struct CfgW1 : Plain<0, 99> { static constexpr int R(int pos) { return pos <= 32 ? pos : imin(40, 2 * pos - 32); } };
struct CfgKV : Plain<4, 99> { static constexpr int R(int pos) { return imin(64, imax(pos, imin(8 + 2 * pos, KVFL))); } };
struct CfgQ : Plain<16, 99> { static constexpr int R(int pos) { return imin(pos, 32); } };
struct CfgW3 : Plain<20, 99> { static constexpr int R(int pos) { return imin(112 + NPRE4, imax(imax(NPRE, pos), 2 * pos - (124 - NPRE4))); } };
template <class CFG> constexpr int want_at(int pos) { for (int n = CFG::R(pos); n < CFG::R(pos + 1); ++n) if (CFG::want(n)) return CFG::want(n); return 0; }
template <class CFG, int NU, int LAG, int START, class RD, class EX, class MM>
DEV void stream(const Sy& y, u32 pre, RD&& rd, EX&& ex, MM&& mm) {
  static_assert(!want_at<CFG>(START + 1) && !want_at<CFG>(START + 2) && !want_at<CFG>(START + 3), "");
  u64 mk = 0;
  if constexpr (want_at<CFG>(START) > 0) mk = chk1(pre);
  sfor<START, NU + LAG>([&](auto I) {
    constexpr int pos = I.value;
    if constexpr (want_at<CFG>(pos) > 0) {
      chk2<want_at<CFG>(pos) - 1>(y.vl, mk);
    }
    if constexpr (((pos - START) & 7) == 0 && pos - START < NU) prof<CFG::first + ((pos - START) >> 3)>(y);
    if constexpr (want_at<CFG>(pos + 4) > 0) pre = peek_l<want_at<CFG>(pos + 4) - 1>(y.vl);
    sfor<CFG::R(pos), CFG::R(pos + 1)>([&](auto N) { rd(N); if constexpr (CFG::done(N.value)) count<Y_CONS>(y.vn, y.nc); });
    if constexpr (want_at<CFG>(pos + 1) > 0) mk = chk1(pre);
    fence_sched();
    ex(I);
    fence_sched();
    if constexpr (pos >= LAG) mm(IC<pos - LAG>{});
    fence_sched();
  });
}

struct Args {
  const void *x, *wqkv, *wo, *w1, *w2, *w3, *w4;
  void *out, *ws;
};

#if defined(__HIP_DEVICE_COMPILE__)
DEV u32 cluster_base(const Args& a, int c) { return WS_CL - ((u32)(u64)a.ws & 0xfffffu) + c * CL; }
DEV void compute(const Args& a, u32 lb, int w, int c, int j, u32 vo0, u32 so0) {
  STAMP_DECL;
  STAMP(0);

  const int l = threadIdx.x & 63, tok = l & 31, hh = l >> 5, g = (l >> 4) & 1, qq = (l >> 2) & 3, p = l & 3;
  const int m = w & 1, u = w >> 1;
  const int s1 = j, skv = j, s2 = j, sq = j;
  const rsrc_t rx = mkrs(a.x, 2048 * 1024), rw = mkrs(a.ws, WS_END), ro = mkrs(a.out, 2048 * 1024);
  u32 cb = cluster_base(a, c);
  v4u d0[4][4];
  const int G0 = w + 4 * (j & XROT);
  auto ldx = [&](auto I) { constexpr int i = I.value >> 2, k = I.value & 3;
    d0[i][k] = __builtin_amdgcn_raw_buffer_load_b128(rx, vo0 + k * 256, i ? (c * 64 + ((4 * i + G0) & 15) * 4) * 1024 : so0, 0); };
  sfor<0, 8>(ldx);
  fence_sched();
  u32 warm;
  {
    u64 pc; u32 len;
    asm volatile("s_getpc_b64 %0\n.Lhere_%=:\n\ts_mov_b32 %1, .Lcode_end-.Lhere_%=" : "=s"(pc), "=s"(len));
    const u32 b = blockIdx.x, sh = (0x7cf7u >> (4 * w)) & 15u;
    u32 o0 = b * 2048, o1 = (b >> 3) * 1024 + ((0x180u - ((u32)(u64)a.w1 & 0x380u)) & 0x380u), o2 = (b >> 3) * 128, o3 = (c * 64 + (j & 2) * 33 - 2) * 1024;
    asm("" : "+s"(o0), "+s"(o1), "+s"(o2), "+s"(o3));
    o0 = (w & 1) ? o1 : o0; o2 = (w & 1) ? o3 : o2;
    asm("" : "+s"(o0), "+s"(o2));
    const u32 vt = pin((l << sh) + ((w & 2) ? o2 : o0));
    warm = __builtin_amdgcn_raw_buffer_load_b32(mkrs(w == 2 ? (const void*)pc : w == 3 ? a.x : a.w1, w == 2 ? len : w == 3 ? 2048 * 1024 : 512 * 1024),
                                                 l < 16 ? vt : OOB, 0, 0);
  }
  u32 wm[5] = {0, 0, 0, 0, 0};
  auto share = [&](auto M) {
    auto t = [&](const void* p, int n) { return __builtin_amdgcn_raw_buffer_load_b32(mkrs(p, n * 131072), l < n ? l * 128 : OOB, (blockIdx.x * 4 + w) * n * 128, 0); };
    if constexpr (M.value & 1) wm[0] = t(a.wqkv, 12);
    if constexpr (M.value & 2) wm[1] = t(a.w2, 4);
    if constexpr (M.value & 4) wm[2] = t(a.w3, 14);
    if constexpr (M.value & 8) { wm[3] = t(a.wo, 4); wm[4] = t(a.w4, 14); } };
  share(IC<PT0>{});
  sfor<8, 10>(ldx);
  fence_sched();
  Sy y;
  y.va = pin(lb + L_SYNC + 4 * (l & 3)); y.vn = pin(lb + L_SYNC + 4 * w); y.nc = y.nl = y.nx = 0;
  y.vl = pin(lb + L_LAND + 252 + 512 * (l & 1));
  const u32 va = y.va, vp = pin(lb + L_SYNC + 4 * (w ^ 2));
  const u32 pb = 16 * (p & 1) + 8 * (p >> 1);
  const u32 l16 = pin(l * 16), voob = pin(OOB);
  auto stored = [&](auto E) { zero_behind(rw, lb + L_EV + u * 320 + 32 + (m ? -4 : 4) * E.value, voob); };
  const u32 wA = pin((8 * hh + qq) * 128 + (((2 * u + g) ^ qq) * 32) + pb);
  const LB bA = mklb(lb + wA);
  const u32 xrow = pin(lb + (2 + 32 * m + tok) * PROW + hh * 16);
  const u32 rrow = pin(lb + L_R1 + (4 + 32 * m + tok) * 4);
  const int gs = s1 >> 1, sh = (gs & 1 ? 1 : -1) * (gs & 2 ? 2 : 1);
  const u32 xsh = pin(xrow + sh * PROW);
  u32 pw[4];
  sfor<0, 4>([&](auto I) { pw[I.value] = pin(lb + (2 + (l >> 4) + 4 * ((4 * I.value + G0) & 15)) * PROW + (l & 15) * 16); });
  fence_sched();
  float vr[4];
  sfor<0, 2>([&](auto H) { constexpr int h = 2 * H.value;
    float s[2];
    s[0] = sum32(d0[h]);
    if constexpr (h == 0) { fence_sched(); STAMP(26); sfor<10, 13>(ldx); fence_sched(); }
    s[1] = sum32(d0[h + 1]);
    if constexpr (h == 0) { fence_sched(); sfor<13, 16>(ldx); share(IC<PT1>{}); post<Y_GO, 1>(y.vn); STAMP(1); }
    fence_sched();
    row_sum(s);
    sfor<0, 8>([&](auto I) { constexpr int i = h + (I.value >> 2), k = I.value & 3;
      d0[i][k] = center8(d0[i][k], s[I.value >> 2] * (-1.0f / 512.0f));
      if constexpr (h == 0 || LNORD) lds128w(pw[i] + k * 256, d0[i][k]); });
    fence_sched();
  });
  sfor<0, 4>([&](auto I) { constexpr int i = I.value;
    if constexpr (!LNORD) {
      lds128w(pw[2 + (i >> 1)] + (i & 1) * 512, d0[2 + (i >> 1)][2 * (i & 1)]);
      lds128w(pw[2 + (i >> 1)] + (i & 1) * 512 + 256, d0[2 + (i >> 1)][2 * (i & 1) + 1]);
    }
    if constexpr (i == (LNORD ? 0 : 3)) { count<Y_LN>(y.vn, y.nl); STAMP(24); }
    fence_sched();
    vr[i] = sq32(d0[i]);
    fence_sched(); });
  pin(warm);
  row_sum(vr);
  { const u32 v0 = pin(f2u(vr[0])), v1 = pin(f2u(vr[1])), v2 = pin(f2u(vr[2])), v3 = pin(f2u(vr[3]));
    put_scales(lb, 2 + ((4 * p + G0) & 15) * 4 + (l >> 4), qq, u2f(p & 2 ? (p & 1 ? v3 : v2) : (p & 1 ? v1 : v0)) * (1.0f / 512.0f));
    count<Y_LN>(y.vn, y.nl); }
  STAMP(2);
  u32 pre = peek_l<0>(y.vl);
  { const u32 ph = peek<Y_HALO>(va), pl = peek<Y_LN>(va); spin_pre<Y_LN, 1>(va, pl); spin_pre<Y_HALO, 1>(va, ph); }
  STAMP(3);

  v4u wkv[64];
  auto rdkv = [&](auto I) { constexpr int n = I.value, o = slot_of(4 + (n >> 3)) + (n & 1) * 8192 + ((n >> 1) & 3) * 2048;
    wkv[n] = tr2(at<o>(bA), at<o + 512>(bA)); };
  float r1;
  {
    f32x16 acc = {};
    v4u wf[32], xf[32];
    float r12 = 0.f;
    GK gk;
    u32 pl = 0;
    stream<CfgW1, 32, 4, 0>(y, pre,
        [&](auto I) { constexpr int n = I.value, o = slot_of(n >> 3) + (n & 7) * 2048;
          if constexpr (n < 32) { wf[n] = tr2(at<o>(bA), at<o + 512>(bA)); xf[n] = lds128(xsh + n * 32); } else rdkv(IC<n - 32>{}); },
        [&](auto I) { constexpr int pos = I.value;
          if constexpr (pos == 13) pl = peek<Y_LN>(va);
          if constexpr (pos == 18) { spin_pre<Y_LN, 2>(va, pl); r12 = *(LAS const float*)(rrow + 576 + sh * 4); r1 = *(LAS const float*)rrow; }
          if constexpr (pos == 26) { gk = gelu_k(r12); gk.x = u2f(pin(f2u(gk.x))); gk.y = u2f(pin(f2u(gk.y))); gk.z = u2f(pin(f2u(gk.z))); } },
        [&](auto I) { constexpr int n = I.value; acc = mfma(wf[n], xf[n], acc); });
    STAMP(4);
    sfor<0, 5>([&](auto K) { pin(wm[K.value]); });
    const u32 so = cb + C_H + (m * 32 + 4 * s1 + 2 * u) * 1024;
    st128<SC1>(gelu8<0>(acc, gk), rw, l16, so);
    pre = peek_l<5>(y.vl);
    fence_sched();
    st128<SC1>(gelu8<8>(acc, gk), rw, l16 + 1024, so);
    stored(IC<1>{});
    fence_sched();
  }
  STAMP(5);
  v4u af[16], kv0, kv1;
  f32x16 res;
  float zs;
  {
    f32x16 acc[2] = {};
    v4u xf[32];
    f32x4 rt[4], rl[4];
    v4u n0, n1;
    u32 pr = 0;
    const u32 rb = pin(lb + L_R1 + (4 + 32 * m + 4 * hh) * 4);
    stream<CfgKV, 64, 5, 0>(y, pre, rdkv,
        [&](auto I) { constexpr int pos = I.value;
          if constexpr (pos < 64 && !(pos & 1)) xf[pos >> 1] = lds128(xrow + pos * 16);
          if constexpr (pos >= 64 && pos < 68) { rt[pos - 64] = *(LAS const f32x4*)(rb + (pos - 64) * 32); rl[pos - 64] = *(LAS const f32x4*)(rb + 288 + (pos - 64) * 32); }
          if constexpr (pos == 61) { n0 = lds128(xrow + (8 * s2 + 4 * u) * 16); n1 = lds128(xrow + (8 * s2 + 4 * u) * 16 + 32); }
          if constexpr (pos >= 65) sfor<2 * (pos - 65), 2 * (pos - 64)>([&](auto K) { constexpr int k = K.value; const u32 v = k < 4 ? n0[k & 3] : n1[k & 3];
            res[2 * k] = u2f(pin(f2u(blo(v) * r1))); res[2 * k + 1] = u2f(pin(f2u(bhi(v) * r1))); });
          if constexpr (pos == 66) { pr = peek<Y_RDYA>(va); pre = peek_l<12>(y.vl); } },
        [&](auto I) { constexpr int n = I.value; acc[n & 1] = mfma(xf[n >> 1], wkv[n], acc[n & 1]); });
    STAMP(6);
    spin_pre<Y_RDYA, 1>(va, pr);
    STAMP(7);
    const u32 so = cb + C_H + m * 32768 + u * 4096;
    auto ldf = [&](auto I) { constexpr int n = I.value; af[n] = __builtin_amdgcn_raw_buffer_load_b128(rw, l16 + (n & 3) * 1024, so + (n >> 2) * 8192, SC1); };
    sfor<0, 4>(ldf);
    const u32 vote = peek<Y_PI>(va);
    fence_sched();
    sfor<0, 8>([&](auto I) { constexpr int i = 2 * I.value;
      ldf(IC<4 + I.value>{});
      sfor<i, i + 2>([&](auto K) { constexpr int k = K.value; acc[0][k] = phi(acc[0][k], rt[k >> 2][k & 3], rl[k >> 2][k & 3]); acc[1][k] *= rt[k >> 2][k & 3]; });
      fence_sched(); });
    ldf(IC<12>{});
    f32x16 kv = mfma(pack8<0>(acc[0]), pack8<0>(acc[1]), f32x16{});
    fence_sched();
    ldf(IC<13>{});
    kv = mfma(pack8<8>(acc[0]), pack8<8>(acc[1]), kv);
    fence_sched();
    ldf(IC<14>{});
    f32x2 z2[2] = {{acc[0][0], acc[0][1]}, {acc[0][2], acc[0][3]}};
    sfor<2, 8>([&](auto I) { z2[I.value & 1] += f32x2{acc[0][2 * I.value], acc[0][2 * I.value + 1]}; });
    z2[0] += z2[1];
    zs = swap32_add(z2[0].x + z2[0].y);
    fence_sched();
    ldf(IC<15>{});
    cb += __builtin_amdgcn_readfirstlane(vote);
    sfor<0, 4>([&](auto I) { const u32 h0 = pin(f2u(kv[8 + I.value])), h1 = pin(f2u(kv[12 + I.value]));
      kv0[I.value] = g ? h0 : f2u(kv[I.value]); kv1[I.value] = g ? h1 : f2u(kv[4 + I.value]); });
    fence_sched();
  }
  auto kvst = [&](auto K) {
    const u32 sp = WS_KVP + (4 * skv + 2 * u) * 73728 + (2 * c + m) * 1152, vo = g * 73728 + (hh * 4 + qq) * 128 + p * 16;
    if constexpr (K.value == 0) st128<SC1>(kv0, rw, vo, sp);
    if constexpr (K.value == 2) st128<SC1>(kv1, rw, vo + 64, sp);
    if constexpr (K.value == 4) __builtin_amdgcn_raw_buffer_store_b32(f2u(zs), rw, g * 73728 + 1024 + ((qq & 1) * 8 + (qq >> 1) * 4 + p) * 4 + hh * OOB, sp, SC1);
  };
  STAMP(8);
  const LB bO = mklb(lb + wA + u * 8192), bX = mklb(lb + (wA ^ 64) + u * 8192);
  auto add16 = [&](f32x16& own, u32 ar) {
    sfor<0, 4>([&](auto I) { constexpr int i = I.value; const v4u v = lds128(ar + i * 1024);
      own[4 * i] += u2f(v.x); own[4 * i + 1] += u2f(v.y); own[4 * i + 2] += u2f(v.z); own[4 * i + 3] += u2f(v.w); });
  };
  v4u x1k[2];
  {
    typedef Tail<12, 32, 99, 0> T;
    f32x16 acc[2] = {{}, res};
    const u32 aw = pin(lb + IC<slot_of(13)>::value + w * 4096 + l * 16), ar = pin(lb + IC<slot_of(13)>::value + (w ^ 2) * 4096 + l * 16);
    v4u wf[32];
    u32 pc = 0;
    stream<T, 32, 6, 0>(y, pre,
        [&](auto I) { constexpr int n = I.value, o = slot_of(12 + (T::ks(n) >> 2)) + (T::ks(n) & 3) * 2048;
          if constexpr (T::own(n)) wf[n] = tr2(at<o>(bO), at<o + 512>(bO)); else wf[n] = tr2(at<o>(bX), at<o + 512>(bX));
          if constexpr (n == 31) count<Y_CONS>(y.vn, y.nc); },
        [&](auto I) { constexpr int pos = I.value;
          if constexpr (KVEARLY) { kvst(IC<pos - KVST>{}); if constexpr (pos == KVEV) stored(IC<2>{}); }
          if constexpr (pos == 24) pc = peek<Y_CONS>(va);
          if constexpr (pos == 28) spin_pre<Y_CONS, 14>(va, pc);
          if constexpr (pos == 32 || pos == 33) { lds128w(aw + (2 * pos - 64) * 1024, raw4<8 * pos - 256>(acc[0])); lds128w(aw + (2 * pos - 63) * 1024, raw4<8 * pos - 252>(acc[0])); }
          if constexpr (pos == 33) count<Y_XCH>(y.vn, y.nx);
          if constexpr (pos == 36) pc = peek<Y_XCH>(vp); },
        [&](auto I) { constexpr int n = I.value; acc[T::own(n)] = mfma(wf[n], af[T::ks(n)], acc[T::own(n)]); });
    STAMP(9);
    spin_pre<Y_XCH, 1>(vp, pc);
    add16(acc[1], ar);
    pre = peek_l<16>(y.vl);
    if constexpr (!C16) count<Y_CONS>(y.vn, y.nc);
    x1k[0] = pack8<0>(acc[1]); x1k[1] = pack8<8>(acc[1]);
    const u32 so = cb + C_X1 + sp((m * 32 + 4 * s2 + 2 * u) * 1024);
    st128<SC1>(x1k[0], rw, l16, so);
    st128<SC1>(x1k[1], rw, l16 + 1024, so);
    fence_sched();
    if constexpr (C16 == 1) count<Y_CONS>(y.vn, y.nc);
    f32x2 t[8], s[2];
    sfor<0, 8>([&](auto I) { t[I.value] = f32x2{acc[1][2 * I.value], acc[1][2 * I.value + 1]}; });
    s[0] = (t[0] + t[1]) + (t[2] + t[3]); s[1] = (t[4] + t[5]) + (t[6] + t[7]); s[0] += s[1];
    const float sm = swap32_add(s[0].x + s[0].y), mn = sm * (1.0f / 32.0f);
    sfor<0, 8>([&](auto I) { t[I.value] -= f32x2{mn, mn}; });
    s[0] = t[0] * t[0]; s[1] = t[1] * t[1];
    sfor<2, 8>([&](auto I) { s[I.value & 1] = __builtin_elementwise_fma(t[I.value], t[I.value], s[I.value & 1]); });
    s[0] += s[1];
    const float m2 = swap32_add(s[0].x + s[0].y);
    __builtin_amdgcn_raw_buffer_store_b64(v2u{f2u(sm), f2u(m2)}, rw, hh ? OOB : tok * 8, cb + C_ST + (2 * s2 + u) * 512 + m * 256, SC1);
    stored(IC<2 + KVEARLY>{});
    if constexpr (C16 == 2) count<Y_CONS>(y.vn, y.nc);
    fence_sched();
  }
  STAMP(10);
  {
    f32x16 acc = {};
    v4u wf[32], xf[32];
    stream<CfgQ, 32, 4, 0>(y, pre,
        [&](auto I) { constexpr int n = I.value, o = slot_of(16 + (n >> 3)) + (n & 7) * 2048;
          wf[n] = tr2(at<o>(bA), at<o + 512>(bA)); xf[n] = lds128(xrow + n * 32); },
        [&](auto I) { if constexpr (!KVEARLY) { kvst(IC<I.value - KVST>{}); if constexpr (I.value == KVST + 4) stored(IC<3>{}); } },
        [&](auto I) { constexpr int n = I.value; acc = mfma(wf[n], xf[n], acc); });
    const float r1l = r1 * LOG2E;
    sfor<0, 16>([&](auto I) { constexpr int i = I.value; acc[i] = phi(acc[i], r1, r1l); });
    const u32 ad = lb + L_HX + (m * 4 + 2 * u) * 1024 + l16;
    lds128w(ad, pack8<0>(acc));
    lds128w(ad + 1024, pack8<8>(acc));
    fence_sched();
  }
  STAMP(11);
  const u32 vx = (32 * m + tok) * 1024 + hh * 16, sx = c * 65536 + (8 * s2 + 4 * u) * 16;
  constexpr int WIN = WIN4;
  v4u hf[72], w4f[144], o0, o1, x0, x1;
  auto ldx2 = [&] { x0 = __builtin_amdgcn_raw_buffer_load_b128(rx, vx, sx, 0); x1 = __builtin_amdgcn_raw_buffer_load_b128(rx, vx + 32, sx, 0); };
  typedef Tail<34, 144, 44, NPRE4> T4;
  auto rd4 = [&](auto I) { constexpr int n = I.value, o = slot_of(34 + (T4::ks(n) >> 2)) + (T4::ks(n) & 3) * 2048;
    if constexpr (T4::own(n)) w4f[n] = tr2(at<o>(bO), at<o + 512>(bO)); else w4f[n] = tr2(at<o>(bX), at<o + 512>(bX)); };
  auto ldh = [&](auto I) { constexpr int i = I.value;
    if constexpr (i < 56) hf[i] = __builtin_amdgcn_raw_buffer_load_b128(rw, l16 + (i & 3) * 1024, cb + C_H2 + (m * 112 + 56 * u) * 2048 + IC<sp(line_of(i >> 2) * 4096)>::value, SC1);
    else hf[i] = __builtin_amdgcn_raw_buffer_load_b128(rw, l16 + (i & 3) * 1024, cb + C_O + m * 65536 + u * 4096 + ((i - 56) >> 2) * 16384, SC1); };
  {
    constexpr int LAG = 6, N0 = NPRE;
    const int fl0 = (7 * j + 1) >> 1, hcol = (j & 1) ? 64 * fl0 - 32 : 64 * (fl0 + 3);
    const LB bE = mklb(lb + wA + u * 2048), bD = mklb(lb + wA - u * 2048 + 2048);
    const u32 bH = lb + (8 * hh + qq) * 64 + ((g ^ u) * 32) + pb + u * 1024;
    const u32 bH0 = pin(bH + IC<slot_of(20)>::value), bH1 = pin(bH + IC<slot_of(21)>::value);
    const u32 fw = pin(lb + w * 16384 + l * 16), fr = pin(lb + (w ^ 2) * 16384 + l * 16);
    auto so = [&](int P) { return cb + C_H2 + sp((m * 112 + 4 * (fl0 + P) + 2 * u) * 1024); };
    const u32 sH = cb + C_H2 + sp((m * 112 + (hcol >> 4) + u) * 1024);
    const u32 sg = cb + C_X1 + m * 65536 + u * 1024;
    v4u xa[32], wf[112], e0, e1;
    f32x16 acc[4] = {};
    float ct[8];
    u32 pk = 0;
    auto rd = [&](auto I) { constexpr int n = I.value;
      if constexpr (n < 16) wf[n] = tr2((n < 8 ? bH0 : bH1) + (n & 7) * 2048, (n < 8 ? bH0 : bH1) + (n & 7) * 2048 + 256);
      else if constexpr (n >= 112) rd4(IC<n - 112>{});
      else { constexpr int o = slot_of(20 + (n >> 3)) + (n & 6) * 2048;
        if constexpr (n & 1) wf[n] = tr2(at<o>(bD), at<o + 512>(bD)); else wf[n] = tr2(at<o>(bE), at<o + 512>(bE)); } };
    sfor<0, N0 / 8>([&](auto P) { spin_l<20 + P.value, 0>(y.vl); sfor<8 * P.value, 8 * P.value + 8>(rd); count<Y_CONS>(y.vn, y.nc); fence_sched(); });
    STAMP(16);
    spin<Y_CONS, 20, 0>(va);
    spin<Y_RDYA, 2, 0>(va);
    STAMP(12);
    auto ldg = [&](auto I) { constexpr int i = I.value;
      xa[2 * i] = __builtin_amdgcn_raw_buffer_load_b128(rw, l16 + (i & 1) * 2048, sg + IC<sp((i >> 1) * 4096)>::value, SC1); };
    sfor<0, XW>(ldg);
    fence_sched();
    spin<Y_STAT, 1, 0>(va);
    const float nm = *(LAS const float*)(lb + L_NM3 + (32 * m + tok) * 4);
    GK gk;
    float r3 = 0.f;
    STAMP(13);
    xa[0] = center8(xa[0], nm); lds128w(fw, xa[0]);
    fence_sched();
    center_a(xa[2], nm, ct);
    stream<CfgW3, 112, LAG, LAG>(y, 1, rd,
        [&](auto I) { constexpr int pos = I.value, q = pos - LAG;
          if constexpr (q < 15) { xa[2 * q + 2] = center_b(ct); lds128w(fw + (q + 1) * 1024, xa[2 * q + 2]); }
          if constexpr (q == 6 || q == 14) count<Y_LN>(y.vn, y.nl);
          if constexpr (q == 8 || q == 16) pk = peek<Y_LN>(vp);
          if constexpr (q == 10) spin_pre<Y_LN, 3>(vp, pk);
          if constexpr (q == 18) spin_pre<Y_LN, 4>(vp, pk);
          if constexpr (q >= 11 && q < 19) xa[2 * (q - 11) + 1] = lds128(fr + (q - 11) * 1024);
          if constexpr (q >= 19 && q < 23) { xa[4 * q - 59] = lds128(fr + (2 * q - 30) * 1024); xa[4 * q - 57] = lds128(fr + (2 * q - 29) * 1024); }
          if constexpr (pos == 31) count<Y_CONS>(y.vn, y.nc);
          if constexpr (XW + q < 16) ldg(IC<XW + q>{});
          fence_sched();
          if constexpr (q < 14) center_a(xa[2 * q + 4], nm, ct);
          if constexpr (q == 17) { lds128w(fr, raw4<8>(acc[3])); lds128w(fr + 1024, raw4<12>(acc[3])); count<Y_XCH>(y.vn, y.nx); }
          if constexpr (XAT == 1 && q == 13) ldx2();
          if constexpr (q == 12) pk = peek<Y_STAT>(va);
          if constexpr (q == 15) { spin_pre<Y_STAT, 2>(va, pk); r3 = *(LAS const float*)(lb + L_R3 + (32 * m + tok) * 4); }
          if constexpr (q == 23) gk = gelu_k(r3);
          if constexpr (q == 21) pk = peek<Y_XCH>(vp);
          if constexpr (q == 24) { spin_pre<Y_XCH, 2>(vp, pk); e0 = lds128(fw); e1 = lds128(fw + 1024); }
          if constexpr (q == 28) { sfor<0, 4>([&](auto K) { acc[3][K.value] += u2f(e0[K.value]); acc[3][4 + K.value] += u2f(e1[K.value]); });
            st128<SC1>(gelu8<0>(acc[3], gk), rw, l16, sH); }
          if constexpr (q == 49 || q == 81) { constexpr int P = q == 81;
            st128<SC1>(gelu8<0>(acc[P], gk), rw, l16, so(P)); fence_sched();
            st128<SC1>(gelu8<8>(acc[P], gk), rw, l16 + 1024, so(P)); stored(IC<4 + P>{}); }
        },
        [&](auto I) { constexpr int n = I.value;
          if constexpr (n < 16) acc[3] = mfma(wf[n], xa[2 * n], acc[3]);
          else acc[(n - 16) >> 5] = mfma(wf[n], xa[(n - 16) & 31], acc[(n - 16) >> 5]); });
    STAMP(14);
    st128<SC1>(gelu8<0>(acc[2], gk), rw, l16, so(2));
    const u32 pa = peek<Y_RDYA>(va), pb_ = peek<Y_RDYB>(va);
    fence_sched();
    st128<SC1>(gelu8<8>(acc[2], gk), rw, l16 + 1024, so(2));
    stored(IC<6>{});
    fence_sched();
    STAMP(17);
    spin_pre<Y_RDYB, 1>(va, pb_);
    const u32 ko = lb + L_KF + u * 2048 + ((l & 16) ? (hh * 4 + qq) * 128 + p * 16 : hh * 128 + 64), po = lb + L_HX + (m * 4 + 2 * u) * 1024 + l16;
    const v4u k0 = lds128(ko), k1 = lds128(ko + 1024), p0 = lds128(po), p1 = lds128(po + 1024);
    spin_pre<Y_RDYA, 3>(va, pa);
    STAMP(18);
    sfor<0, 3>(ldh);
    fence_sched();
    f32x16 n0 = mfma(k0, p0, f32x16{}), n1 = mfma(k1, p1, f32x16{});
    fence_sched();
    sfor<3, 6>(ldh);
    fence_sched();
    const float d0_ = __builtin_amdgcn_rcpf(n0[0] + 1e-6f), d1_ = __builtin_amdgcn_rcpf(n1[0] + 1e-6f);
    sfor<8, 16>([&](auto K) { n0[K.value] *= d0_; n1[K.value] *= d1_; });
    o0 = pack8<8>(n0); o1 = pack8<8>(n1);
    fence_sched();
    sfor<6, WIN>(ldh);
    fence_sched();
  }
  STAMP(15);
  {
    typedef T4 T;
    constexpr int LAG = 6;
    f32x16 acc[4] = {}, xs;
    sfor<0, 8>([&](auto I) { constexpr int i = I.value; acc[1][2 * i] = blo(x1k[i >> 2][i & 3]); acc[1][2 * i + 1] = bhi(x1k[i >> 2][i & 3]); });
    const u32 aw = pin(lb + IC<slot_of(43)>::value + w * 4096 + l * 16), ar = pin(lb + IC<slot_of(43)>::value + (w ^ 2) * 4096 + l * 16);
    v4u ye[4];
    const float nl2e = -LOG2E, one = 1.0f;
    u32 pc = 0;
    STAMP(19);
    if (((CP4 >> w) & 1) != 0) __builtin_amdgcn_s_setprio(2);
    stream<T, 144, LAG, LAG>(y, 1, rd4,
        [&](auto I) { constexpr int pos = I.value, q = pos - LAG, nx = q / 2 - 1 + WIN;
          if constexpr (q == OST) st128<SC1>(o0, rw, l16, cb + C_O + sp((m * 32 + 4 * sq + 2 * u) * 1024));
          if constexpr (q == OST + 4) { st128<SC1>(o1, rw, l16 + 1024, cb + C_O + sp((m * 32 + 4 * sq + 2 * u) * 1024)); stored(IC<7>{}); }
          if constexpr ((q & 1) && nx == 55) spin_pre<Y_RDYA, 6>(va, pc);
          if constexpr ((q & 1) && (nx == 21 || nx == 37 || nx == 53)) pc = peek<Y_RDYA>(va);
          if constexpr ((q & 1) && nx == 39) spin_pre<Y_RDYA, 5>(va, pc);
          if constexpr ((q & 1) && nx == 23) spin_pre<Y_RDYA, 4>(va, pc);
          if constexpr (q == 103) pc = peek<Y_CONS>(va);
          if constexpr (q == 107) spin_pre<Y_CONS, 45>(va, pc);
          if constexpr (XAT == 0 && q == 61) ldx2();
          if constexpr (q >= 2 && !(q & 1) && q < 112) ldh(IC<nx>{});
          if constexpr (q >= 97 && q < 112 && (q & 1)) { constexpr int i = (q - 97) / 2; const u32 v = i < 4 ? x0[i & 3] : x1[i & 3];
            xs[2 * i] = u2f(pin(v << 16)); xs[2 * i + 1] = u2f(pin(v & 0xffff0000u)); }
          if constexpr (q == 112 || q == 113) { lds128w(aw + (2 * q - 224) * 1024, raw4<8 * q - 896>(acc[0])); lds128w(aw + (2 * q - 223) * 1024, raw4<8 * q - 892>(acc[0])); }
          if constexpr (q == 113) count<Y_XCH>(y.vn, y.nx);
          if constexpr (q >= 112 && 55 + WIN + (q - 112) < 72) ldh(IC<55 + WIN + (q - 112)>{});
          if constexpr (q == 110 + SILU) pc = peek<Y_XCH>(vp);
          if constexpr (q == 113 + SILU) { spin_pre<Y_XCH, 3>(vp, pc); sfor<0, 4>([&](auto K) { ye[K.value] = lds128(ar + K.value * 1024); }); count<Y_XCH>(y.vn, y.nx); }
          if constexpr (q >= 116 + SILU && q < 124 + SILU && !((q - SILU) & 1)) { constexpr int i = (q - 116 - SILU) / 2;
            float yv[4], e[4];
            sfor<0, 4>([&](auto K) { yv[K.value] = a_add(acc[1][4 * i + K.value], u2f(ye[i][K.value])); });
            sfor<0, 4>([&](auto K) { e[K.value] = a_mul(yv[K.value], nl2e); });
            sfor<0, 4>([&](auto K) { e[K.value] = a_exp(e[K.value]); });
            sfor<0, 4>([&](auto K) { acc[1][4 * i + K.value] = yv[K.value]; ye[i][K.value] = f2u(e[K.value]); }); }
          if constexpr (q >= 116 + SILU && q < 124 + SILU && ((q - SILU) & 1)) { constexpr int i = (q - 116 - SILU) / 2;
            float e[4];
            sfor<0, 4>([&](auto K) { e[K.value] = a_add(u2f(ye[i][K.value]), one); });
            sfor<0, 4>([&](auto K) { e[K.value] = a_rcp(e[K.value]); });
            sfor<0, 4>([&](auto K) { acc[1][4 * i + K.value] = a_mul(acc[1][4 * i + K.value], e[K.value]); }); }
          if constexpr (q == 130) pc = peek<Y_XCH>(vp);
          if constexpr (q == 134) spin_pre<Y_XCH, 4>(vp, pc);
          if constexpr (q == 138 || q == 139) { lds128w(aw + (2 * q - 276) * 1024, raw4<8 * q - 1104>(acc[2])); lds128w(aw + (2 * q - 275) * 1024, raw4<8 * q - 1100>(acc[2])); }
          if constexpr (q == 139) count<Y_XCH>(y.vn, y.nx);
          if constexpr (q == 142) pc = peek<Y_XCH>(vp); },
        [&](auto I) { constexpr int n = I.value, k = (n >= 112 ? 2 : 0) + T::own(n); acc[k] = mfma(w4f[n], hf[T::ks(n)], acc[k]); });
    STAMP(20);
    spin_pre<Y_XCH, 5>(vp, pc);
    add16(acc[3], ar);
    sfor<0, 16>([&](auto K) { acc[3][K.value] = __builtin_fmaf(acc[3][K.value], acc[1][K.value], xs[K.value]); });
    if constexpr (OUTM == 0) {
      st128<OUTAUX>(pack8<0>(acc[3]), ro, vx, sx);
      st128<OUTAUX>(pack8<8>(acc[3]), ro, vx + 32, sx);
    } else if constexpr (OUTM == 1) {
      const u32 tb = lb + L_HX + (m * 4 + 2 * u) * 1024, sw_ = ((tok >> 1) & 3) * 16;
      lds128w(tb + tok * 64 + ((hh * 16) ^ sw_), pack8<0>(acc[3]));
      lds128w(tb + tok * 64 + ((32 + hh * 16) ^ sw_), pack8<8>(acc[3]));
      const u32 rd_ = tb + (l >> 2) * 64 + ((p * 16) ^ (((l >> 3) & 3) * 16));
      const v4u t0 = lds128(rd_), t1 = lds128(rd_ + 1024);
      const u32 vo = (32 * m + (l >> 2)) * 1024 + p * 16;
      st128<OUTAUX>(t0, ro, vo, sx);
      st128<OUTAUX>(t1, ro, vo + 16384, sx);
    } else {
      const u32 tb = lb + L_HX + m * 4096, sw_ = (tok & 7) * 16;
      lds128w(tb + tok * 128 + ((u * 64 + hh * 16) ^ sw_), pack8<0>(acc[3]));
      lds128w(tb + tok * 128 + ((u * 64 + 32 + hh * 16) ^ sw_), pack8<8>(acc[3]));
      count<Y_XCH>(y.vn, y.nx);
      spin<Y_XCH, 6, 0>(vp);
      const u32 rd_ = tb + u * 2048 + (l >> 3) * 128 + (((l & 7) ^ (l >> 3)) * 16);
      const v4u t0 = lds128(rd_), t1 = lds128(rd_ + 1024);
      const u32 vo = (32 * m + 16 * u + (l >> 3)) * 1024 + (l & 7) * 16;
      st128<OUTAUX>(t0, ro, vo, c * 65536 + s2 * 128);
      st128<OUTAUX>(t1, ro, vo + 8192, c * 65536 + s2 * 128);
    }
  }
  STAMP(21);
  STAMP(22);
  STAMP_OUT(a.ws, w);
}

DEV void loader(const Args& a, u32 lb, int j, int k) {
  STAMP_DECL;
  const int l = threadIdx.x & 63;
  const int s1 = j, skv = j, s2 = j, sq = j;
  const u32 vc = lb + L_SYNC + Y_CONS + 4 * (l & 3);
  const u32 sw = ((((l >> 1) & 3) ^ ((l >> 3) & 3)) * 32) + (l & 1) * 16;
  const int fl0 = (7 * j + 1) >> 1, hcol = (j & 1) ? 64 * fl0 - 32 : 64 * (fl0 + 3);
  STAMP(0);
  asm volatile(
      ".macro SEG ptr, bytes, row, col, stride, step\n\t"
      "s_mov_b64 s[64:65], \\ptr\n\t"
      "s_and_b32 s65, s65, 0xffff\n\t"
      "s_mov_b32 s66, \\bytes\n\t"
      "s_movk_i32 s68, \\stride\n\t"
      "v_mad_u32_u24 v10, \\row, s68, \\col\n\t"
      "v_add_u32 v11, \\step, v10\n\t"
      "v_add_u32 v12, 2*\\step, v10\n\t"
      "v_add_u32 v13, 3*\\step, v10\n\t"
      ".endm\n\t"
      ".macro GAP on, f\n\t"
      ".if \\on\n\t"
      ".if \\f & 16\n\t"
      "buffer_load_dwordx2 v[6:7], v15, s[64:67], 0 offen\n\t"
      ".else\n\t"
      "buffer_load_dword v6, v15, s[64:67], 0 offen\n\t"
      ".endif\n\t"
      ".endif\n\t"
      ".endm\n\t"
      ".macro QUAD f\n\t"
      "buffer_load_dwordx4 v10, s[64:67], s68 offen lds\n\t"
      "GAP (\\f & 1), \\f\n\t"
      "buffer_load_dwordx4 v11, s[64:67], s68 offen offset:1024 lds\n\t"
      "GAP (\\f & 2), \\f\n\t"
      "buffer_load_dwordx4 v12, s[64:67], s68 offen offset:2048 lds\n\t"
      "GAP (\\f & 4), \\f\n\t"
      "buffer_load_dwordx4 v13, s[64:67], s68 offen offset:3072 lds\n\t"
      "GAP (\\f & 8), \\f\n\t"
      ".endm\n\t"
      ".macro FREE req, p\n\t"
      "s_waitcnt lgkmcnt(0)\n\t"
      "v_cmp_gt_u32 vcc, \\req, v14\n\t"
      "s_cbranch_vccz 1f\n\t"
      "s_movk_i32 s70, \\req\n\t"
      "s_call_b64 s[72:73], .Lwait_%=\n"
      "1:\n\t"
      ".endm\n\t"
      ".macro PIECE p, slot, base, off, q1, cap, f\n\t"
      "s_add_u32 m0, %[dst], \\slot\n\t"
      "s_add_u32 s68, \\base, \\off\n\t"
      "s_waitcnt vmcnt(\\cap)\n\t"
      "QUAD \\f\n\t"
      "s_add_u32 m0, %[dst], \\slot+4096\n\t"
      "s_add_u32 s68, \\base, \\off+\\q1\n\t"
      "s_waitcnt vmcnt(\\cap)\n\t"
      "QUAD \\f\n\t"
      "s_add_u32 m0, %[land], 4*\\p\n\t"
      "ds_read_b32 v14, %[vc]\n\t"
      "buffer_load_dword v15, s[64:67], 0 offen lds\n\t"
      ".endm\n\t"
      ".macro RQUAD r, base, off, cap\n\t"
      "s_add_u32 s68, \\base, \\off\n\t"
      "s_waitcnt vmcnt(\\cap)\n\t"
      "buffer_load_dwordx4 v[\\r:\\r+3], v10, s[64:67], s68 offen\n\t"
      "buffer_load_dwordx4 v[\\r+4:\\r+7], v11, s[64:67], s68 offen offset:1024\n\t"
      "buffer_load_dwordx4 v[\\r+8:\\r+11], v12, s[64:67], s68 offen offset:2048\n\t"
      "buffer_load_dwordx4 v[\\r+12:\\r+15], v13, s[64:67], s68 offen offset:3072\n\t"
      ".endm\n\t"
      ".macro RSTORE p, slot, r, vm\n\t"
      "v_add_u32 v16, \\slot, v17\n\t"
      "s_waitcnt vmcnt(\\vm)\n\t"
      ".irp i, 0, 1, 2, 3, 4, 5, 6, 7\n\t"
      "ds_write_b128 v16, v[\\r+4*\\i:\\r+4*\\i+3] offset:1024*\\i\n\t"
      ".endr\n\t"
      "ds_write_b32 v18, v19 offset:4*\\p\n\t"
      "ds_read_b32 v14, %[vc]\n\t"
      ".endm\n\t"
      ".macro WORD off, val\n\t"
      "s_mov_b32 s74, %[spin]\n"
      "5:\n\t"
      "ds_read_b32 v16, v9 offset:\\off\n\t"
      "s_waitcnt lgkmcnt(0)\n\t"
      "v_cmp_gt_u32 vcc, \\val, v16\n\t"
      "s_cbranch_vccz 6f\n\t"
      "s_sleep 1\n\t"
      "s_sub_u32 s74, s74, 1\n\t"
      "s_cbranch_scc0 5b\n"
      "6:\n\t"
      ".endm\n\t"
      "v_lshl_add_u32 v17, %[rH], 6, %[cH]\n\t"
      "v_add_u32 v17, %[dst], v17\n\t"
      "v_mov_b32 v18, %[land]\n\t"
      "v_add_u32 v18, 252, v18\n\t"
      "v_mov_b32 v19, 0\n\t"
      "v_subrev_u32 v9, %[ycons], %[vc]\n\t"
      "s_mov_b32 s67, 0x20000\n\t"
      "v_mov_b32 v15, %[oob]\n\t"
      "s_branch .Lgo_%=\n"
      ".Lwait_%=:\n\t"
      "s_mov_b32 s74, %[spin]\n"
      "2:\n\t"
      "ds_read_b32 v14, %[vc]\n\t"
      "s_waitcnt lgkmcnt(0)\n\t"
      "v_cmp_gt_u32 vcc, s70, v14\n\t"
      "s_cbranch_vccz 4f\n\t"
      "s_sub_u32 s74, s74, 1\n\t"
      "s_cbranch_scc0 2b\n"
      "4:\n\t"
      "s_setpc_b64 s[72:73]\n"
      ".Lgo_%=:\n\t"
      "WORD 128, 1\n\t"
      "SEG %[w1], 524288, %[rA], %[sw], 1024, 7168\n\t"
      "PIECE 0, 81920, %[b1], 0, 32768, 27, 26\n\t"
      "PIECE 1, 98304, %[b1], 131072, 32768, 27, 26\n\t"
      "PIECE 2, 114688, %[b1], 262144, 32768, 27, 26\n\t"
      "PIECE 3, 131072, %[b1], 393216, 32768, 27, 26\n\t"
      "SEG %[wq], 1572864, %[rA], %[sw], 3072, 23552\n\t"
      "PIECE 4, 147456, %[bkv], 0, 98304, 27, 26\n\t"
      "FREE 1, 5\n\t"
      "PIECE 5, 81920, %[bkv], 196608, 98304, 21, 10\n\t"
      "FREE 2, 6\n\t"
      "PIECE 6, 98304, %[bkv], 393216, 98304, 21, 10\n\t"
      "FREE 3, 7\n\t"
      "PIECE 7, 114688, %[bkv], 589824, 98304, 21, 10\n\t"
      "FREE 4, 8\n\t"
      "PIECE 8, 131072, %[bkv], 786432, 98304, 21, 10\n\t"
      "FREE 5, 9\n\t"
      "PIECE 9, 147456, %[bkv], 983040, 98304, 21, 10\n\t"
      "FREE 6, 10\n\t"
      "PIECE 10, 81920, %[bkv], 1179648, 98304, 21, 10\n\t"
      "FREE 7, 11\n\t"
      "PIECE 11, 98304, %[bkv], 1376256, 98304, 21, 10\n\t"
      "SEG %[w2], 524288, %[rA], %[sw], 1024, 7168\n\t"
      "FREE 8, 12\n\t"
      "PIECE 12, 114688, %[b2], 0, 32768, 21, 10\n\t"
      "FREE 9, 13\n\t"
      "PIECE 13, 131072, %[b2], 131072, 32768, 21, 10\n\t"
      "FREE 10, 14\n\t"
      "PIECE 14, 147456, %[b2], 262144, 32768, 21, 10\n\t"
      "FREE 11, 15\n\t"
      "PIECE 15, 81920, %[b2], 393216, 32768, 21, 10\n\t"
      "SEG %[wq], 1572864, %[rA], %[sw], 3072, 23552\n\t"
      "FREE 12, 16\n\t"
      "PIECE 16, 98304, %[bq], 0, 98304, 21, 10\n\t"
      "FREE 13, 17\n\t"
      "PIECE 17, 114688, %[bq], 393216, 98304, 28, 31\n\t"
      "FREE 15, 18\n\t"
      "PIECE 18, 147456, %[bq], 786432, 98304, 21, 10\n\t"
      "FREE 15, 19\n\t"
      "PIECE 19, 81920, %[bq], 1179648, 98304, 21, 10\n\t"
      "SEG %[w3], 1835008, %[rH], %[cH], 3584, 56320\n\t"
      "FREE 16, 20\n\t"
      "PIECE 20, 131072, %[bH], 0, 229376, 12, 10\n\t"
      "FREE 17, 21\n\t"
      "PIECE 21, 98304, %[bH], 917504, 229376, 12, 10\n\t"
      "SEG %[w3], 1835008, %[rA], %[sw], 3584, 27648\n\t"
      "FREE 18, 22\n\t"
      "PIECE 22, 114688, %[b3], 0, 114688, 12, 10\n\t"
      "FREE 19, 23\n\t"
      "PIECE 23, 147456, %[b3], 458752, 114688, 21, 26\n\t"
      "WORD 16, 1\n\t"
      "FREE 20, 24\n\t"
      "PIECE 24, 81920, %[b3], 917504, 114688, 21, 26\n\t"
      "FREE 21, 25\n\t"
      "PIECE 25, 131072, %[b3], 1376256, 114688, 21, 26\n\t"
      "FREE 22, 26\n\t"
      "PIECE 26, 98304, %[b3], 128, 114688, 21, 26\n\t"
      "FREE 23, 27\n\t"
      "PIECE 27, 114688, %[b3], 458880, 114688, 21, 26\n\t"
      "FREE 24, 28\n\t"
      "PIECE 28, 147456, %[b3], 917632, 114688, 21, 26\n\t"
      "FREE 25, 29\n\t"
      "PIECE 29, 0, %[b3], 1376384, 114688, 24, 10\n\t"
      "FREE 25, 30\n\t"
      "PIECE 30, 16384, %[b3], 256, 114688, 24, 10\n\t"
      "FREE 25, 31\n\t"
      "PIECE 31, 32768, %[b3], 459008, 114688, 24, 10\n\t"
      "FREE 25, 32\n\t"
      "PIECE 32, 49152, %[b3], 917760, 114688, 24, 10\n\t"
      "FREE 26, 33\n\t"
      "PIECE 33, 81920, %[b3], 1376512, 114688, 24, 10\n\t"
      "SEG %[w4], 1835008, %[rA], %[sw], 1024, 7168\n\t"
      "FREE 27, 34\n\t"
      "PIECE 34, 131072, %[b4], 196608, 32768, 24, 10\n\t"
      "FREE 28, 35\n\t"
      "PIECE 35, 98304, %[b4], 655360, 32768, 24, 10\n\t"
      "FREE 29, 36\n\t"
      "PIECE 36, 114688, %[b4], 0, 32768, 24, 10\n\t"
      "FREE 30, 37\n\t"
      "PIECE 37, 147456, %[b4], 262144, 32768, 24, 10\n\t"
      "FREE 31, 38\n\t"
      "PIECE 38, 0, %[b4], 458752, 32768, 24, 10\n\t"
      "FREE 32, 39\n\t"
      "PIECE 39, 16384, %[b4], 720896, 32768, 24, 10\n\t"
      "FREE 33, 40\n\t"
      "PIECE 40, 32768, %[b4], 65536, 32768, 24, 10\n\t"
      "FREE 34, 41\n\t"
      "PIECE 41, 49152, %[b4], 327680, 32768, 24, 10\n\t"
      "FREE 35, 42\n\t"
      "PIECE 42, 81920, %[b4], 524288, 32768, 24, 10\n\t"
      "FREE 36, 43\n\t"
      "PIECE 43, 131072, %[b4], 786432, 32768, 16, 0\n\t"
      "FREE 37, 44\n\t"
      "PIECE 44, 98304, %[b4], 131072, 32768, 16, 0\n\t"
      "FREE 38, 45\n\t"
      "PIECE 45, 114688, %[b4], 393216, 32768, 16, 0\n\t"
      "FREE 39, 46\n\t"
      "PIECE 46, 147456, %[b4], 589824, 32768, 16, 0\n\t"
      "FREE 40, 47\n\t"
      "PIECE 47, 0, %[b4], 851968, 32768, 16, 0\n\t"
      "SEG %[wo], 524288, %[rA], %[sw], 1024, 7168\n\t"
      "FREE 41, 48\n\t"
      "PIECE 48, 16384, %[b2], 0, 32768, 16, 0\n\t"
      "FREE 42, 49\n\t"
      "PIECE 49, 32768, %[b2], 131072, 32768, 16, 0\n\t"
      "FREE 43, 50\n\t"
      "PIECE 50, 49152, %[b2], 262144, 32768, 16, 0\n\t"
      "FREE 44, 51\n\t"
      "PIECE 51, 81920, %[b2], 393216, 32768, 16, 0\n\t"
      "s_waitcnt vmcnt(0) lgkmcnt(0)\n\t"
      ".purgem SEG\n\t.purgem QUAD\n\t.purgem GAP\n\t.purgem PIECE\n\t.purgem FREE\n\t.purgem RQUAD\n\t.purgem RSTORE\n\t.purgem WORD"
      :
      : [w1] "s"(a.w1), [wq] "s"(a.wqkv), [w2] "s"(a.w2), [w3] "s"(a.w3), [wo] "s"(a.wo), [w4] "s"(a.w4),
        [rA] "v"(l >> 3), [sw] "v"(sw), [rH] "v"(l >> 2), [cH] "v"((l & 3) * 16), [vc] "v"(vc), [land] "s"(lb + L_LAND + 512 * k), [oob] "i"(OOB),
        [dst] "s"(lb + k * 8192), [b1] "s"(s1 * 128 + k * 65536), [bkv] "s"(1024 + skv * 128 + k * 1024), [b2] "s"(s2 * 128 + k * 65536),
        [b4] "s"(s2 * 128 + k * 14 * 65536), [bq] "s"(sq * 128 + k * 64 * 3072), [b3] "s"(fl0 * 128 + k * 64 * 3584), [bH] "s"(hcol * 2 + k * 128 * 3584),
        [ws] "s"(a.ws), [bc] "s"(0x2800000u + 0x400000u + j * 0x200000u + k * 16384u + ((((__builtin_amdgcn_s_getreg(20 | (3 << 11)) & 15) >> 2) ^ j ^ PI0) & 1) * 8192u), [ycons] "i"(Y_CONS), [spin] "i"(SPIN_LDS)
      : "memory", "vcc", "scc", "s64", "s65", "s66", "s67", "s68", "s70", "s72", "s73", "s74", "s98", "s99",
        "v6", "v7", "v8", "v9", "v10", "v11", "v12", "v13", "v14", "v15", "v16", "v17", "v18", "v19");
  STAMP(9);
  STAMP_OUT(a.ws, 4 + 3 * k);
}

typedef GAS u64 gu64;
DEV u64 read_tag(const Args& a) { return __hip_atomic_load((gu64*)a.ws, RLX_AGENT) + 0x9E3779B97F4A7C15ull; }
template <int SLEEP> DEV u32 poll(gu64* word, bool on, u64 tag) {
  u32 s;
#pragma unroll 1
  for (s = 0; s < SPIN_GLB; ++s) {
    const u64 v = on ? __hip_atomic_load(word, RLX_AGENT) : tag;
    if (__all(v == tag)) break;
    __builtin_amdgcn_s_sleep(SLEEP);
  }
  asm volatile("" ::: "memory");
  return s;
}
DEV void warm_scalar(const void* base, int n, u32& t) {
  const char* p = (const char*)base;
#pragma unroll 1
  for (int i = 0; i < n; ++i) {
    asm volatile(
        "s_load_dword %0, %1, 0\n\ts_load_dword %0, %1, 64\n\ts_load_dword %0, %1, 128\n\ts_load_dword %0, %1, 192\n\t"
        "s_load_dword %0, %1, 256\n\ts_load_dword %0, %1, 320\n\ts_load_dword %0, %1, 384\n\ts_load_dword %0, %1, 448"
        : "+s"(t) : "s"(p) : "memory");
    p += 512;
  }
}
DEV void warm_done(u32& t) { asm volatile("s_waitcnt lgkmcnt(0)" : "+s"(t) :: "memory"); }
template <int OFF> DEV u32 probe(const char* p) {
  u64 t0, t1; u32 d;
  asm volatile("s_memtime %0\n\ts_load_dword %2, %3, %4\n\ts_waitcnt lgkmcnt(0)\n\ts_memtime %1\n\ts_waitcnt lgkmcnt(0)"
               : "=&s"(t0), "=&s"(t1), "=&s"(d) : "s"(p), "i"(OFF) : "memory");
  return (u32)t1 - (u32)t0;
}
template <int E, int SLEEP> DEV void wait_stored(u32 lb) {
  const int l = threadIdx.x;
  spin_l<0, SLEEP>(lb + L_EV + (l & 2) * 160 + 32 + (l & 1 ? -4 * E : 252 + 4 * E));
}
template <int S0, int S1> DEV u32 poll8(rsrc_t rw, u32 so, u32 vld, u64 tag) {
  u64 r; u32 c;
  asm volatile(
      "s_mov_b32 %1, %6\n\t"
      "s_setprio %9\n\t"
      "s_sleep %7\n"
      "0:\n\t"
      "buffer_load_dwordx2 %0, %2, %3, %4 offen sc1\n\t"
      "s_waitcnt vmcnt(0)\n\t"
      "v_cmp_ne_u64 vcc, %0, %5\n\t"
      "s_cbranch_vccz 1f\n\t"
      "s_sleep %8\n\t"
      "s_sub_u32 %1, %1, 1\n\t"
      "s_cbranch_scc0 0b\n"
      "1:"
      : "=&v"(r), "=&s"(c) : "v"(vld), "s"(rw), "s"(so), "v"(tag), "i"(SPIN_GLB), "i"(S0), "i"(S1), "i"(FPRIO) : "memory", "vcc", "scc");
  return SPIN_GLB - c;
}
DEV void comm_a(const Args& a, u32 lb, int c, int j) {
  STAMP_DECL;
  const int l = threadIdx.x & 63, b = blockIdx.x;
  const u32 va = lb + L_SYNC + 4 * (l & 3);
  const rsrc_t rw = mkrs(a.ws, WS_END);
  const int n32 = b >> 3;
  u32 cb = cluster_base(a, c);
  auto touch = [&](const void* p, u32 o) { return *(const GAS u32*)((u64)p + o); };
  auto slow = [&](const void* p) { return (0x180u - ((u32)(u64)p & 0x380u)) & 0x380u; };
  u32 w0 = 0, w1 = 0, ta = ~0u, tb = ~0u;
  const char* pp = (const char*)a.ws + cb + C_PR + j * 1024;
  const u32 r32 = n32 + 32 * (l & 15);
  const u64 tag = read_tag(a);
  {
    constexpr int PT = PT0 | PT1;
    const u32 o1 = pin(b * 2048 + (l & 15) * 128), o3 = pin(r32 * 1024 + slow(a.w2)), o4 = pin(r32 * 3072 + (l >> 4) * 1024 + slow(a.wqkv));
    const u32 o5 = pin(cb + C_FL + (l >> 1) * 1024 + (l & 1) * 8192 + j * 128), o6 = pin(WS_KVF + (l & 3) * 32768 + (c * 8 + j) * 128), o7 = pin(b * 7168 + (l < 56 ? l : 0) * 128);
    probe<896>(pp);
    if (NPRB > 2) { ta = probe<0>(pp); tb = probe<8192>(pp); }
    STAMP(1);
    spin_l<T1AT, 1>(lb + L_LAND + 252);
    w0 = touch(l < 48 ? a.wqkv : a.w2, l < 48 ? o4 : o3);
    if (!(PT & 2)) w1 = touch(a.w2, o1);
    const u32 w2 = __builtin_amdgcn_raw_buffer_load_b32(rw, l < 12 ? o5 : o6, 0, SC1);
    const u32 w3 = (PT & 4) ? 0 : touch(a.w3, o7);
    u32 w4 = 0, w5 = 0;
    if (TMODE == 2) { w4 = touch(a.wo, o1); w5 = touch(a.w4, o7); }
    post<Y_WARM, 1>(va);
    STAMP(3);
    fence_sched();
    tb = min(tb, probe<8192 + 256>(pp)); ta = min(ta, probe<256>(pp));
    if (NPRB > 1) { ta = min(ta, probe<512>(pp)); tb = min(tb, probe<8192 + 512>(pp)); }
    fence_sched();
    asm volatile("" :: "v"(w0), "v"(w1), "v"(w2), "v"(w3), "v"(w4), "v"(w5));
  }
  STAMP(10);
  u32 vst = pin(l == 0 ? cb + j * 128 : OOB), vld = pin(cb + (l & 7) * 128);
  const u32 cnt = min(max(((int)(ta - tb) + 400) / 100, 0), 7);
  STAMP(7);
  auto flag = [&](auto E, auto S) {
    wait_stored<E.value, WSL>(lb);
    __builtin_amdgcn_s_setprio(FPRIO);
    __builtin_amdgcn_raw_buffer_store_b64(__builtin_bit_cast(v2u, tag), rw, vst, C_FL + S.value * 1024, SC1);
    fence_sched();
    __builtin_amdgcn_s_setprio(0);
    STAMP(S.value < 5 ? 2 * S.value : 9);
  };
  auto ready = [&](auto S, auto SL) {
    poll8<SL.value, PS1>(rw, C_FL + S.value * 1024, vld, tag);
    if constexpr (S.value != 1) post<Y_RDYA, S.value + 1>(va);
    if constexpr (S.value != 1) __builtin_amdgcn_s_setprio(0);
    STAMP(12 + S.value);
  };
  {
    const u64 mine = tag ^ (u64)(PIM == 1 ? (u32)(tb < ta) : PI0 ^ (((__builtin_amdgcn_s_getreg(20 | (3 << 11)) >> 2) ^ (c >> 1)) & 1));
    wait_stored<1, WSL>(lb);
    __builtin_amdgcn_raw_buffer_store_b64(__builtin_bit_cast(v2u, mine), rw, vst, C_FL, SC1);
    STAMP(0);
    spin<Y_CONS, PC0, 1>(va);
    u64 v = 0;
#pragma unroll 1
    for (u32 s = 0; s < SPIN_GLB; ++s) {
      v = __builtin_bit_cast(u64, __builtin_amdgcn_raw_buffer_load_b64(rw, vld, C_FL, SC1)) ^ tag;
      if (!__any(v > 1)) break;
      asm volatile("s_sleep %0" :: "i"(PS1) : "memory");
    }
    const u32 off = __builtin_popcount((u32)__ballot(v == 1) & 255u) > 4 ? 8192u : 0u;
    *(volatile LAS u32*)(va + Y_PI) = off;
    post<Y_RDYA, 1>(va);
    STAMP(12);
    cb += off; vst += off; vld = pin(cb + ((l & 7) == j ? j ^ 1 : l & 7) * 128);
  }
  spin<Y_CONS, T2AT, 1>(va);
  w0 = touch(a.w3, (n32 + 32 * (l & 15)) * 3584 + (7 * (l >> 4) + 3) * 128);
  if (TMODE == 5) { const u32 o1 = pin(b * 2048 + l * 128), o2 = pin(r32 * 1024 + slow(a.wo));
    const u32 t0 = touch(a.wo, l < 16 ? o1 : o2), t1 = touch(a.w4, b * 7168 + (l < 56 ? l : 0) * 128); pin(t0); pin(t1); }
  flag(IC<2 + KVEARLY>{}, IC<1>{});
  w1 = __builtin_amdgcn_raw_buffer_load_b32(rw, cb + sp(j * 4096) + (l >> 5) * 65536 + (l & 31) * 128, C_X1, SC1);
  const u32 w2 = __builtin_amdgcn_raw_buffer_load_b32(rw, l < 8 ? cb + j * 1024 + l * 128 : OOB, C_ST, SC1);
  fence_sched();
  const u32 vs = pin(cb + l * 16);
  ready(IC<1>{}, IC<PS_X1>{});
  {
    v4u d[8];
    sfor<0, 8>([&](auto K) { d[K.value] = __builtin_amdgcn_raw_buffer_load_b128(rw, vs + (K.value & 3) * 1024, C_ST + (K.value >> 2) * 4096, SC1);
      if constexpr (K.value == RDYAT) { fence_sched(); post<Y_RDYA, 2>(va); fence_sched(); } });
    fence_sched();
    float s0 = 0.f, s1 = 0.f, q0 = 0.f, q1 = 0.f;
    sfor<0, 8>([&](auto K) { s0 += u2f(d[K.value].x); s1 += u2f(d[K.value].z); });
    s0 = swap32_add(s0) * (-1.0f / 512.0f); s1 = swap32_add(s1) * (-1.0f / 512.0f);
    *(LAS f32x2*)(lb + L_NM3 + l * 8) = f32x2{s0, s1};
    post<Y_STAT, 1>(va);
    fence_sched();
    STAMP(11);
    float e0 = 0.f, e1 = 0.f;
    sfor<0, 8>([&](auto K) { const float t0 = __builtin_fmaf(s0, 32.0f, u2f(d[K.value].x)), t1 = __builtin_fmaf(s1, 32.0f, u2f(d[K.value].z));
      e0 = __builtin_fmaf(t0, t0, e0); e1 = __builtin_fmaf(t1, t1, e1); q0 += u2f(d[K.value].y); q1 += u2f(d[K.value].w); });
    q0 = swap32_add(__builtin_fmaf(e0, 1.0f / 32.0f, q0)); q1 = swap32_add(__builtin_fmaf(e1, 1.0f / 32.0f, q1));
    *(LAS f32x2*)(lb + L_R3 + (l & 31) * 8) = f32x2{__builtin_amdgcn_rsqf(__builtin_fmaf(q0, 1.0f / 512.0f, 1e-5f)), __builtin_amdgcn_rsqf(__builtin_fmaf(q1, 1.0f / 512.0f, 1e-5f))};
    post<Y_STAT, 2>(va);
    __builtin_amdgcn_s_setprio(0);
  }
  pin(w0); pin(w1);
  pin(w2);
  if (TMODE == 6) { __builtin_amdgcn_s_sleep(T6SL); pin(touch(a.w4, b * 7168 + (l < 56 ? l : 0) * 128)); }
  if (TMODE < 2 && !((PT0 | PT1) & 8)) { w0 = touch(a.wo, b * 2048 + (l & 15) * 128); w1 = touch(a.w4, b * 7168 + (l < 56 ? l : 0) * 128); pin(w0); pin(w1); }
  w0 = touch(a.w4, (n32 + 32 * (l < 56 ? l : 0)) * 1024 + slow(a.w4));
  flag(IC<4>{}, IC<2>{});
  flag(IC<5>{}, IC<3>{});
  ready(IC<2>{}, IC<0>{});
  pin(w0);
  flag(IC<6>{}, IC<4>{});
  ready(IC<3>{}, IC<0>{});
  flag(IC<7>{}, IC<5>{});
  if (TMODE < 5 || TMODE >= 7) w0 = touch(a.wo, (n32 + 32 * (l & 15)) * 1024 + slow(a.wo));
  ready(IC<4>{}, IC<PS_C>{});
  ready(IC<5>{}, IC<PS_D>{});
  pin(w0);
  if (b == 0 && l == 0) __hip_atomic_store((gu64*)a.ws, tag, RLX_AGENT);
  STAMP_OUT(a.ws, 5);
}
DEV void comm_b(const Args& a, u32 lb, int c, int j) {
  STAMP_DECL;
  const int l = threadIdx.x & 63, b = blockIdx.x;
  const int skv = j, sq = j;
  const u32 va = lb + L_SYNC + 4 * (l & 3), vc = va;
  const rsrc_t rw = mkrs(a.ws, WS_END);
  gu64* fl = (gu64*)((char*)a.ws + WS_FG);
  gu64* fo = fl + (c * 8 + j) * 16;
  {
    const int r = l >> 4, prow = r < 2 ? r : 64 + r;
    const u32 vo = ((r ^ j) & 2) ? OOB : (c * 64 - 2 + prow) * 1024 + (l & 15) * 16;
    v4u d[4];
    sfor<0, 4>([&](auto K) { d[K.value] = __builtin_amdgcn_raw_buffer_load_b128(mkrs(a.x, 2048 * 1024), vo + K.value * 256, 0, 0); });
    if (KVT0 == 3) { fence_sched(); u32 t = 0; warm_scalar((const char*)a.wqkv + b * 6144 + 4096, 4, t); warm_done(t); fence_sched(); }
    float s[1] = {sum32(d)};
    row_sum(s);
    sfor<0, 4>([&](auto K) { d[K.value] = center8(d[K.value], s[0] * (-1.0f / 512.0f)); lds128w(lb + prow * PROW + (l & 15) * 16 + K.value * 256, d[K.value]); });
    s[0] = sq32(d);
    row_sum(s);
    put_scales(lb, prow, l & 15, s[0] * (1.0f / 512.0f));
    post<Y_HALO, 1>(va);
  }
  const u64 tag = read_tag(a);
  STAMP(8);
  if (TMODE == 3 || TMODE == 4) { u32 t = 0; warm_scalar((const char*)a.w4 + b * 7168, 14, t); warm_scalar((const char*)a.wo + b * 2048, 4, t); warm_done(t); }
  if (TMODE >= 7) { u32 t = 0; __builtin_amdgcn_s_sleep(SC3S); warm_scalar((const char*)a.w3 + b * 7168, 14, t); warm_done(t); }
  STAMP(9);
#define BAIL do { post<Y_RDYB, 1>(va); STAMP_OUT(a.ws, 6); return; } while (0)
  wait_stored<3 - KVEARLY, 2>(lb);
  STAMP(0);
  if (l == 0) __hip_atomic_store(fo + F_KP, tag, RLX_AGENT);
  spin<Y_CONS, PCKP, 2>(va);
  __builtin_amdgcn_s_setprio(OPRIO);
  poll<12>(fl + F_KP + ((l & 31) * 8 + (c >> 2)) * 16, l < 32, tag);
  STAMP(1);
  {
    v4u d[8], zz[2];
    sfor<0, 8>([&](auto I) { d[I.value] = __builtin_amdgcn_raw_buffer_load_b128(rw, (l >> 3) * 1152 + (l & 7) * 16, WS_KVP + c * 73728 + j * 128 + I.value * 9216, SC1); });
    sfor<0, 2>([&](auto I) { zz[I.value] = __builtin_amdgcn_raw_buffer_load_b128(rw, j < 2 ? (l >> 1) * 1152 + (l & 1) * 16 : OOB, WS_KVP + c * 73728 + 1024 + j * 32 + I.value * 36864, SC1); });
    fence_sched();
    f32x4 s = __builtin_bit_cast(f32x4, d[0]), z = __builtin_bit_cast(f32x4, zz[0]) + __builtin_bit_cast(f32x4, zz[1]);
    sfor<1, 8>([&](auto I) { s += __builtin_bit_cast(f32x4, d[I.value]); });
    sfor<0, 4>([&](auto I) { s[I.value] = swap32_add(swap16_add(dpp_add<0x128>(s[I.value])));
      z[I.value] = swap32_add(swap16_add(dpp_add<0x122>(dpp_add<0x124>(dpp_add<0x128>(z[I.value]))))); });
    if (l & 8) s = z;
    __builtin_amdgcn_raw_buffer_store_b64(v2u{pk2(s[0], s[1]), pk2(s[2], s[3])}, rw, l < 8 ? (l & 3) * 16 + (l >> 2) * 8 : l < 16 ? l * 8 : OOB, WS_KVF + (c * 8 + j) * 128, SC1);
    drain();
    if (l == 0) __hip_atomic_store(fo + F_KF, tag, RLX_AGENT);
    __builtin_amdgcn_s_setprio(0);
  }
  STAMP(2);
  {
    spin<Y_CONS, PCKF, 4>(va);
    poll<12>(fl + F_KF + (sq * 32 + (l & 31)) * 16, l < 32, tag);
    STAMP(3);
    v4u kf[4];
    sfor<0, 4>([&](auto I) { kf[I.value] = __builtin_amdgcn_raw_buffer_load_b128(rw, l * 16 + I.value * 1024, WS_KVF + sq * 4096, SC1); });
    sfor<0, 4>([&](auto I) { lds128w(lb + L_KF + I.value * 1024 + l * 16, kf[I.value]); });
    post<Y_RDYB, 1>(va);
  }
  STAMP(4);
  STAMP_OUT(a.ws, 6);
}

#endif
__global__ void __launch_bounds__(512, 2) fused_block(Args a) {
#if defined(__HIP_DEVICE_COMPILE__)
  __shared__ __attribute__((aligned(16))) char lds[163840];
  const u32 lb = (u32)(size_t)(LAS char*)lds;
  const int w = __builtin_amdgcn_readfirstlane(threadIdx.x >> 6);
  const int b = blockIdx.x, j = (b >> 3) & 7, c = (b & 7) * 4 + (b >> 6);
  asm volatile(".subsection 2\n.Lcode_end:\n\t.fill 256, 4, 0xBF800000\n\t.subsection 0");
  { const u32 z = threadIdx.x >= 29; lds128w(lb + L_SYNC + threadIdx.x * 16, v4u{z, z, z, z}); }
  u32 vo0 = ((threadIdx.x >> 4) & 3) * 1024 + (threadIdx.x & 15) * 16, so0 = (c * 64 + (w + 4 * (j & XROT)) * 4) * 1024;
  asm volatile("s_waitcnt lgkmcnt(0)\n\ts_barrier" : "+v"(vo0), "+s"(so0) : "s"(a.x), "s"(a.ws) : "memory");
  if (w >= 4) {
    if (w == WA) comm_a(a, lb, c, j); else if (w != WB) loader(a, lb, j, w != 4); else comm_b(a, lb, c, j);
  } else compute(a, lb, w, c, j, vo0, so0);
#endif
}
}

extern "C" void kernel_launch(void* const* d_in, const int* in_sizes, int n_in, void* d_out, int out_size, void* d_ws,
                              size_t ws_size, hipStream_t stream) {
  Args a;
  a.x = d_in[0]; a.wqkv = d_in[1]; a.wo = d_in[2]; a.w1 = d_in[4]; a.w2 = d_in[5]; a.w3 = d_in[6]; a.w4 = d_in[7];
  a.out = d_out; a.ws = d_ws;
  fused_block<<<256, 512, 0, stream>>>(a);
}
```
